```python
import torch

def _softmax(x, axis=-1):
    return torch.softmax(x, dim=axis)
def _gelu(x):
    return torch.nn.functional.gelu(x, approximate="tanh")
def _silu(x):
    return torch.nn.functional.silu(x)
def _sigmoid(x):
    return torch.sigmoid(x)
def _layernorm(x, axis=-1, eps=1e-5):
    m = x.mean(dim=axis, keepdim=True)
    v = x.var(dim=axis, keepdim=True, unbiased=False)
    return (x - m) / torch.sqrt(v + eps)
def _scatter_add(tgt, idx, src, axis=0):
    return tgt.clone().index_add_(axis, idx, src)
def _segment_sum(src, seg, n):
    out = torch.zeros((n,) + src.shape[1:], dtype=src.dtype)
    return out.index_add_(0, seg, src)
def _pad(x, pw):
    flat = tuple(v for pair in reversed(pw) for v in pair)
    return torch.nn.functional.pad(x, flat)

kernel_name = 'interleave_layers__softmax_full_mha__dense_activation_gelu__prompt'

def setup_inputs():

    _w = (lambda a: (lambda t: t.to(torch.bfloat16) if t.is_floating_point() else t)(torch.as_tensor(a)))
    def _A_setup():
        B, S, D = 1, 2048, 512
        return {
            "x": _w((lambda *s: torch.randn(*s, dtype=torch.bfloat16) * 0.05)(B, S, D)),
            "wqkv": _w((lambda *s: torch.randn(*s, dtype=torch.bfloat16) * 0.05)(D, 3 * D)),
            "wo": _w((lambda *s: torch.randn(*s, dtype=torch.bfloat16) * 0.05)(D, D)),
        }
    _A_in = _A_setup()

    def _B_setup():
        B, S, D, F = 1, 2048, 512, 1792
        return {
            "x": _w((lambda *s: torch.randn(*s, dtype=torch.bfloat16) * 0.05)(B, S, D)),
            "w1": _w((lambda *s: torch.randn(*s, dtype=torch.bfloat16) * 0.8)(D, F) * (D ** -0.5)),
            "b1": _w((lambda *s: torch.randn(*s, dtype=torch.bfloat16) * 0.2)(F) * 0.01),
            "w2": _w((lambda *s: torch.randn(*s, dtype=torch.bfloat16) * 0.2)(F, D) * (F ** -0.5)),
            "b2": _w((lambda *s: torch.randn(*s, dtype=torch.bfloat16) * 0.2)(D) * 0.01),
        }
    _B_in = _B_setup()

    return {
        "A_x": _A_in["x"],
        "A_wqkv": _A_in["wqkv"],
        "A_wo": _A_in["wo"],
        "B_x": _B_in["x"],
        "B_w1": _B_in["w1"],
        "B_b1": _B_in["b1"],
        "B_w2": _B_in["w2"],
        "B_b2": _B_in["b2"],
    }

def reference(A_x, A_wqkv, A_wo, B_x, B_w1, B_b1, B_w2, B_b2):
    def _A_ref(x, wqkv, wo):
        B, S, D = x.shape
        H = 32; Dh = D // H
        qkv = (lambda x, *d: x.permute(*d))((x @ wqkv).reshape(B, S, 3, H, Dh), 2, 0, 3, 1, 4)
        q, k, v = qkv[0], qkv[1], qkv[2]
        scale = Dh ** -0.5
        scores = torch.einsum("bhqd,bhkd->bhqk", q, k) * scale
        p = _softmax(scores, axis=-1)
        o = torch.einsum("bhqk,bhkd->bhqd", p, v)
        return (lambda x, *d: x.permute(*d))(o, 0, 2, 1, 3).reshape(B, S, D) @ wo

    def _B_ref(x, w1, b1, w2, b2):
        h = torch.einsum("bsd,df->bsf", x, w1) + b1
        h = _gelu(h)
        return torch.einsum("bsf,fd->bsd", h, w2) + b2

    x = A_x
    x = x + _A_ref(_layernorm(x, axis=-1), A_wqkv, A_wo)
    x = x + _B_ref(_layernorm(x, axis=-1), B_w1, B_b1, B_w2, B_b2)
    x = x + _A_ref(_layernorm(x, axis=-1), A_wqkv, A_wo)
    x = x + _B_ref(_layernorm(x, axis=-1), B_w1, B_b1, B_w2, B_b2)
    return x
```

```cpp
#include <hip/hip_runtime.h>
#include <hip/hip_bf16.h>
#include <stdint.h>

using bf16x8 = __attribute__((ext_vector_type(8))) short;
using s16x4  = __attribute__((ext_vector_type(4))) short;
using f32x4  = __attribute__((ext_vector_type(4))) float;
using f32x16 = __attribute__((ext_vector_type(16))) float;
using u32x4  = __attribute__((ext_vector_type(4))) unsigned;
using u32x2  = __attribute__((ext_vector_type(2))) unsigned;
typedef unsigned short u16;
typedef short v4i16_t __attribute__((ext_vector_type(4)));
#define LDSP __attribute__((address_space(3)))
#define DI __device__ __forceinline__

constexpr int S_ = 2048, D_ = 512, H_ = 32, F_ = 1792;
constexpr float LN_EPS = 1e-5f;
constexpr float QSCALE = 0.25f * 1.4426950408889634f;

#define MFMA16(a, b, c) __builtin_amdgcn_mfma_f32_16x16x32_bf16((a), (b), (c), 0, 0, 0)
#define MFMA32(a, b, c) __builtin_amdgcn_mfma_f32_32x32x16_bf16((a), (b), (c), 0, 0, 0)

DI unsigned cvtpk(float lo, float hi) { unsigned r; asm("v_cvt_pk_bf16_f32 %0, %1, %2" : "=v"(r) : "v"(lo), "v"(hi)); return r; }
DI float bf_lo(unsigned u) { return __uint_as_float(u << 16); }
DI float bf_hi(unsigned u) { return __uint_as_float(u & 0xffff0000u); }
DI float rbf(float x) { return bf_lo(cvtpk(x, x)); }
DI s16x4 trread(unsigned addr) {
  return __builtin_bit_cast(s16x4, __builtin_amdgcn_ds_read_tr16_b64_v4i16((LDSP v4i16_t*)(uintptr_t)addr));
}
DI bf16x8 cat8(s16x4 lo, s16x4 hi) { return __builtin_shufflevector(lo, hi, 0, 1, 2, 3, 4, 5, 6, 7); }
DI float gelu_tanh(float x) {
  const float c1 = -2.0f * 0.7978845608028654f * 1.4426950408889634f;
  const float c2 = c1 * 0.044715f;
  float t = x * x;
  float a = x * __builtin_fmaf(t, c2, c1);
  float e = __builtin_amdgcn_exp2f(a);
  return x * __builtin_amdgcn_rcpf(1.0f + e);
}

template <int BN, int EPI>
__global__ __launch_bounds__(256, 1)
void gemm512_kernel(const u16* __restrict__ A, const u16* __restrict__ W,
                    const u16* __restrict__ aux,
                    u16* __restrict__ out0,
                    u16* __restrict__ out1,
                    u16* __restrict__ out2)
{
  constexpr int N = (EPI == 0) ? 1536 : (EPI == 1 ? 512 : F_);
  constexpr int NT = N / BN;
  constexpr int CB = BN / 32;
  constexpr int NF = BN / 16;
  constexpr int NST = 32 * CB;
  constexpr int SPW = NST / 4;
  constexpr bool LN = (EPI != 1);
  extern __shared__ __attribute__((aligned(16))) char smem[];
  float* cs_lds = (float*)(smem + NST * 1024);

  const int tid = threadIdx.x, lane = tid & 63;
  const int w = __builtin_amdgcn_readfirstlane(tid >> 6);
  const int fr = lane & 15, g = lane >> 4;

  const int bid = blockIdx.x, xcd = bid & 7, idx = bid >> 3;
  constexpr int PN = NT / 2;
  const int mt = (xcd >> 1) * 4 + idx / PN;
  const int nt = (xcd & 1) * PN + idx % PN;
  const int m0 = mt * 128 + w * 32;

  u32x4 wst[SPW];
#pragma unroll
  for (int i = 0; i < SPW; ++i) {
    const int st = 4 * i + w, kb = st / CB, cb = st % CB;
    int col;
    if (EPI == 0) col = (cb < 2) ? (64 * nt + 32 * cb) : (1024 + 32 * nt);
    else col = nt * BN + 32 * cb;
    wst[i] = *(const u32x4*)(W + (size_t)(16 * kb + (lane >> 2)) * N + col + 8 * (lane & 3));
  }
  bf16x8 af[2][16];
#pragma unroll
  for (int ks = 0; ks < 16; ++ks)
#pragma unroll
    for (int i = 0; i < 2; ++i) {
      const int row = m0 + 16 * i + fr;
      if (EPI == 1) af[i][ks] = *(const bf16x8*)(A + ((size_t)(2 * ks + (g >> 1)) * S_ + row) * 16 + 8 * (g & 1));
      else af[i][ks] = *(const bf16x8*)(A + (size_t)row * D_ + 32 * ks + 8 * g);
    }
  {
    char* dst = smem + (lane >> 2) * 64 + (((lane & 3) ^ ((lane >> 5) << 1)) << 4);
#pragma unroll
    for (int i = 0; i < SPW; ++i) *(u32x4*)(dst + (4 * i + w) * 1024) = wst[i];
  }
  float mean[2] = {0.f, 0.f}, rstd[2] = {1.f, 1.f};
  if (LN) {
#pragma unroll
    for (int i = 0; i < 2; ++i) {
      float s0 = 0.f, s1 = 0.f, q0 = 0.f, q1 = 0.f;
#pragma unroll
      for (int ks = 0; ks < 16; ++ks) {
        const u32x4 u = __builtin_bit_cast(u32x4, af[i][ks]);
#pragma unroll
        for (int j = 0; j < 4; ++j) {
          const float a = bf_lo(u[j]), b = bf_hi(u[j]);
          s0 += a; s1 += b; q0 = __builtin_fmaf(a, a, q0); q1 = __builtin_fmaf(b, b, q1);
        }
      }
      float s = s0 + s1, q = q0 + q1;
      s += __shfl_xor(s, 16); q += __shfl_xor(q, 16);
      s += __shfl_xor(s, 32); q += __shfl_xor(q, 32);
      const float mu = s * (1.0f / D_);
      const float var = fmaxf(q * (1.0f / D_) - mu * mu, 0.f);
      mean[i] = mu; rstd[i] = __builtin_amdgcn_rsqf(var + LN_EPS);
    }
  }
  __syncthreads();

  const unsigned smem_base = (unsigned)(uintptr_t)smem;
  const int q4 = (lane & 15) >> 2, p4 = lane & 3;
  const unsigned tr_base0 = smem_base + (g >> 1) * CB * 1024 + (8 * (g & 1) + q4) * 64 + ((g & 1) << 5) + 8 * p4;
  const unsigned tr_base1 = smem_base + (g >> 1) * CB * 1024 + (8 * (g & 1) + q4) * 64 + (((g & 1) ^ 1) << 5) + 8 * p4;
  f32x4 acc[2][NF];
#pragma unroll
  for (int nf = 0; nf < NF; ++nf) { acc[0][nf] = f32x4{0.f, 0.f, 0.f, 0.f}; acc[1][nf] = f32x4{0.f, 0.f, 0.f, 0.f}; }
#pragma unroll
  for (int ks = 0; ks < 16; ++ks) {
#pragma unroll
    for (int nf = 0; nf < NF; ++nf) {
      const unsigned a = ((nf & 1) ? tr_base1 : tr_base0) + (2 * ks * CB + (nf >> 1)) * 1024;
      const bf16x8 wf = cat8(trread(a), trread(a + 256));
      if (EPI == 0 && nf >= 4) {
        acc[0][nf] = MFMA16(af[0][ks], wf, acc[0][nf]);
        acc[1][nf] = MFMA16(af[1][ks], wf, acc[1][nf]);
      } else {
        acc[0][nf] = MFMA16(wf, af[0][ks], acc[0][nf]);
        acc[1][nf] = MFMA16(wf, af[1][ks], acc[1][nf]);
      }
    }
  }
  if (LN) {
    bf16x8 ones;
#pragma unroll
    for (int j = 0; j < 8; ++j) ones[j] = (short)0x3F80;
    for (int nf = w; nf < NF; nf += 4) {
      f32x4 c = f32x4{0.f, 0.f, 0.f, 0.f};
      const unsigned b = ((nf & 1) ? tr_base1 : tr_base0) + (nf >> 1) * 1024;
#pragma unroll
      for (int ks = 0; ks < 16; ++ks) {
        const unsigned a = b + 2 * ks * CB * 1024;
        c = MFMA16(ones, cat8(trread(a), trread(a + 256)), c);
      }
      if (g == 0) cs_lds[16 * nf + fr] = c[0];
    }
    __syncthreads();
  }

  if (EPI == 0) {
#pragma unroll
    for (int nf = 0; nf < 4; ++nf) {
      const f32x4 cs = *(const f32x4*)(cs_lds + 16 * nf + 4 * g);
#pragma unroll
      for (int i = 0; i < 2; ++i) {
        const int row = m0 + 16 * i + fr;
        float y[4];
#pragma unroll
        for (int r = 0; r < 4; ++r) y[r] = rstd[i] * (acc[i][nf][r] - mean[i] * cs[r]);
        if (nt < 8) {
          const int head = 4 * nt + nf;
          u32x2 v = {cvtpk(y[0] * QSCALE, y[1] * QSCALE), cvtpk(y[2] * QSCALE, y[3] * QSCALE)};
          *(u32x2*)(out0 + ((size_t)head * S_ + row) * 16 + 4 * g) = v;
        } else {
          const int head = 4 * (nt - 8) + nf;
          u32x2 v = {cvtpk(y[0], y[1]), cvtpk(y[2], y[3])};
          *(u32x2*)(out1 + (((size_t)head * 64 + (row >> 5)) * 2 + (g >> 1)) * 256 + (row & 31) * 8 + 4 * (g & 1)) = v;
        }
      }
    }
#pragma unroll
    for (int i = 0; i < 2; ++i) {
      float mu[4], rs[4];
#pragma unroll
      for (int r = 0; r < 4; ++r) { mu[r] = __shfl(mean[i], 4 * g + r); rs[r] = __shfl(rstd[i], 4 * g + r); }
#pragma unroll
      for (int nf = 4; nf < 6; ++nf) {
        const float cs = cs_lds[16 * nf + fr];
        float y[4];
#pragma unroll
        for (int r = 0; r < 4; ++r) y[r] = rs[r] * (acc[i][nf][r] - mu[r] * cs);
        const int head = 2 * nt + (nf - 4);
        u32x2 v = {cvtpk(y[0], y[1]), cvtpk(y[2], y[3])};
        *(u32x2*)(out2 + ((((size_t)head * 64 + (m0 >> 5)) * 2 + i) * 2 + (g & 1)) * 128 + fr * 8 + 4 * (g >> 1)) = v;
      }
    }
  } else if (EPI == 1) {
#pragma unroll
    for (int i = 0; i < 2; ++i) {
      const int row = m0 + 16 * i + fr;
#pragma unroll
      for (int nf = 0; nf < NF; ++nf) {
        const size_t o = (size_t)row * D_ + nt * BN + 16 * nf + 4 * g;
        const u32x2 xr = *(const u32x2*)(aux + o);
        const float y0 = bf_lo(xr[0]) + rbf(acc[i][nf][0]), y1 = bf_hi(xr[0]) + rbf(acc[i][nf][1]);
        const float y2 = bf_lo(xr[1]) + rbf(acc[i][nf][2]), y3 = bf_hi(xr[1]) + rbf(acc[i][nf][3]);
        u32x2 v = {cvtpk(y0, y1), cvtpk(y2, y3)};
        *(u32x2*)(out0 + o) = v;
      }
    }
  } else {
#pragma unroll
    for (int nf = 0; nf < NF; ++nf) {
      const f32x4 cs = *(const f32x4*)(cs_lds + 16 * nf + 4 * g);
      const int col = nt * BN + 16 * nf + 4 * g;
      const u32x2 br = *(const u32x2*)(aux + col);
      const float b[4] = {bf_lo(br[0]), bf_hi(br[0]), bf_lo(br[1]), bf_hi(br[1])};
#pragma unroll
      for (int i = 0; i < 2; ++i) {
        const int row = m0 + 16 * i + fr;
        float y[4];
#pragma unroll
        for (int r = 0; r < 4; ++r) {
          const float t = rbf(rbf(rstd[i] * (acc[i][nf][r] - mean[i] * cs[r])) + b[r]);
          y[r] = gelu_tanh(t);
        }
        u32x2 v = {cvtpk(y[0], y[1]), cvtpk(y[2], y[3])};
        *(u32x2*)(out0 + (size_t)row * F_ + col) = v;
      }
    }
  }
}

__global__ __launch_bounds__(256, 1)
void ffn2_kernel(const u16* __restrict__ Hh, const u16* __restrict__ W2, const u16* __restrict__ b2,
                 const u16* __restrict__ x1, u16* __restrict__ out)
{
  extern __shared__ __attribute__((aligned(16))) char smem[];
  const int tid = threadIdx.x, lane = tid & 63;
  const int w = __builtin_amdgcn_readfirstlane(tid >> 6);
  const int fr = lane & 15, g = lane >> 4;
  const int bid = blockIdx.x, xcd = bid & 7, idx = bid >> 3;
  const int mt = xcd * 4 + (idx >> 3), nt = idx & 7;
  const int m0 = mt * 64, n0 = nt * 64;
  const int k0 = w * 448;
  constexpr int NS = 14;

  const u16* Ap = Hh + (size_t)(m0 + fr) * F_ + k0 + 8 * g;
  const u16* Wp = W2 + (size_t)(k0 + (lane >> 2)) * D_ + n0 + 8 * (lane & 3);
  char* stg = smem + w * 16384;
  char* dst = stg + (lane >> 2) * 64 + (((lane & 3) ^ ((lane >> 5) << 1)) << 4);
  const unsigned sbase = (unsigned)(uintptr_t)stg;
  const int q4 = (lane & 15) >> 2, p4 = lane & 3;
  const unsigned tr_base0 = sbase + (g >> 1) * 2048 + (8 * (g & 1) + q4) * 64 + ((g & 1) << 5) + 8 * p4;
  const unsigned tr_base1 = sbase + (g >> 1) * 2048 + (8 * (g & 1) + q4) * 64 + (((g & 1) ^ 1) << 5) + 8 * p4;

  bf16x8 af[3][4];
  u32x4 wr[3][4];
  auto issue = [&](int s, int slot) {
#pragma unroll
    for (int st = 0; st < 4; ++st) wr[slot][st] = *(const u32x4*)(Wp + (size_t)(32 * s + 16 * (st >> 1)) * D_ + 32 * (st & 1));
#pragma unroll
    for (int i = 0; i < 4; ++i) af[slot][i] = *(const bf16x8*)(Ap + (size_t)(16 * i) * F_ + 32 * s);
  };
  f32x4 acc[4][4];
#pragma unroll
  for (int i = 0; i < 4; ++i)
#pragma unroll
    for (int nf = 0; nf < 4; ++nf) acc[i][nf] = f32x4{0.f, 0.f, 0.f, 0.f};
  issue(0, 0); issue(1, 1);
#pragma unroll
  for (int s = 0; s < NS; ++s) {
    const int slot = s % 3, buf = (s & 1) * 4096;
    if (s + 2 < NS) issue(s + 2, (s + 2) % 3);
#pragma unroll
    for (int st = 0; st < 4; ++st) *(u32x4*)(dst + buf + st * 1024) = wr[slot][st];
#pragma unroll
    for (int nf = 0; nf < 4; ++nf) {
      const unsigned a = ((nf & 1) ? tr_base1 : tr_base0) + buf + (nf >> 1) * 1024;
      const bf16x8 wf = cat8(trread(a), trread(a + 256));
#pragma unroll
      for (int i = 0; i < 4; ++i) acc[i][nf] = MFMA16(wf, af[slot][i], acc[i][nf]);
    }
  }
  __syncthreads();
  f32x4* red = (f32x4*)smem;
#pragma unroll
  for (int i = 0; i < 4; ++i)
#pragma unroll
    for (int nf = 0; nf < 4; ++nf) red[((w * 4 + i) * 4 + nf) * 64 + lane] = acc[i][nf];
  __syncthreads();
  const int row = m0 + 16 * w + fr;
#pragma unroll
  for (int nf = 0; nf < 4; ++nf) {
    f32x4 sum = red[((0 * 4 + w) * 4 + nf) * 64 + lane];
#pragma unroll
    for (int ww = 1; ww < 4; ++ww) sum += red[((ww * 4 + w) * 4 + nf) * 64 + lane];
    const int col = n0 + 16 * nf + 4 * g;
    const u32x2 br = *(const u32x2*)(b2 + col);
    const size_t o = (size_t)row * D_ + col;
    const u32x2 xr = *(const u32x2*)(x1 + o);
    const float y0 = bf_lo(xr[0]) + rbf(rbf(sum[0]) + bf_lo(br[0]));
    const float y1 = bf_hi(xr[0]) + rbf(rbf(sum[1]) + bf_hi(br[0]));
    const float y2 = bf_lo(xr[1]) + rbf(rbf(sum[2]) + bf_lo(br[1]));
    const float y3 = bf_hi(xr[1]) + rbf(rbf(sum[3]) + bf_hi(br[1]));
    u32x2 v = {cvtpk(y0, y1), cvtpk(y2, y3)};
    *(u32x2*)(out + o) = v;
  }
}

constexpr int ATT_UNR = 4;
constexpr int ATT_LDS = 65536 * 2 + ATT_UNR * 1024;
__global__ __launch_bounds__(512, 2)
void attn_kernel(const u16* __restrict__ Q, const u16* __restrict__ Kf, const u16* __restrict__ Vf,
                 u16* __restrict__ O)
{
  extern __shared__ __attribute__((aligned(16))) char smem[];
  const int tid = threadIdx.x, lane = tid & 63;
  const int w = __builtin_amdgcn_readfirstlane(tid >> 6);
  const int r = lane & 31, h = lane >> 5;
  const int bid = blockIdx.x;
  const int head = (bid & 7) * 4 + ((bid >> 3) & 3), qt = bid >> 5;
  const int q0 = qt * 256 + w * 32;

  {
    const u32x4* ks = (const u32x4*)(Kf + (size_t)head * 32768);
    const u32x4* vs = (const u32x4*)(Vf + (size_t)head * 32768);
    u32x4 kr[8], vr[8];
#pragma unroll
    for (int i = 0; i < 8; ++i) kr[i] = ks[i * 512 + tid];
#pragma unroll
    for (int i = 0; i < 8; ++i) vr[i] = vs[i * 512 + tid];
#pragma unroll
    for (int i = 0; i < 8; ++i) ((u32x4*)smem)[i * 512 + tid] = kr[i];
#pragma unroll
    for (int i = 0; i < 8; ++i) ((u32x4*)(smem + 65536))[i * 512 + tid] = vr[i];
    if (tid < ATT_UNR * 64) ((u32x4*)(smem + 131072))[tid] = u32x4{0x3F803F80u, 0x3F803F80u, 0x3F803F80u, 0x3F803F80u};
  }
  const bf16x8 qf = *(const bf16x8*)(Q + ((size_t)head * S_ + q0 + r) * 16 + 8 * h);
  __syncthreads();

  const char* kp = smem + lane * 16;
  f32x16 zero16;
#pragma unroll
  for (int i = 0; i < 16; ++i) zero16[i] = 0.f;
  float m0 = -INFINITY, m1 = -INFINITY;
  for (int t = 0; t < 64; t += ATT_UNR) {
#pragma unroll
    for (int u = 0; u < ATT_UNR; ++u) {
      const bf16x8 kf = *(const bf16x8*)(kp + (t + u) * 1024);
      const f32x16 s = MFMA32(kf, qf, zero16);
#pragma unroll
      for (int i = 0; i < 16; i += 4) {
        m0 = __builtin_fmaxf(__builtin_fmaxf(s[i], s[i + 1]), m0);
        m1 = __builtin_fmaxf(__builtin_fmaxf(s[i + 2], s[i + 3]), m1);
      }
    }
  }
  float m = __builtin_fmaxf(m0, m1);
  m = __builtin_fmaxf(m, __shfl_xor(m, 32));
  f32x16 negm;
#pragma unroll
  for (int i = 0; i < 16; ++i) negm[i] = -m;

  const char* vp = (r < 16) ? (smem + 65536 + h * 256 + r * 16) : (smem + 131072 + (r - 16) * 16);
  const int vstep = (r < 16) ? ATT_UNR * 1024 : 0;
  f32x16 o = zero16;
  for (int t = 0; t < 64; t += ATT_UNR) {
#pragma unroll
    for (int u = 0; u < ATT_UNR; ++u) {
      const bf16x8 kf = *(const bf16x8*)(kp + (t + u) * 1024);
      const bf16x8 v0 = *(const bf16x8*)(vp + u * 1024);
      const bf16x8 v1 = *(const bf16x8*)(vp + u * 1024 + 512);
      f32x16 s = MFMA32(kf, qf, negm);
#pragma unroll
      for (int i = 0; i < 16; ++i) s[i] = __builtin_amdgcn_exp2f(s[i]);
      u32x4 p0 = {cvtpk(s[0], s[1]), cvtpk(s[2], s[3]), cvtpk(s[4], s[5]), cvtpk(s[6], s[7])};
      u32x4 p1 = {cvtpk(s[8], s[9]), cvtpk(s[10], s[11]), cvtpk(s[12], s[13]), cvtpk(s[14], s[15])};
      o = MFMA32(v0, __builtin_bit_cast(bf16x8, p0), o);
      o = MFMA32(v1, __builtin_bit_cast(bf16x8, p1), o);
    }
    vp += vstep;
  }
  const float inv = 1.0f / o[8];
  u16* op = O + ((size_t)head * S_ + q0 + r) * 16 + 4 * h;
  u32x2 a = {cvtpk(o[0] * inv, o[1] * inv), cvtpk(o[2] * inv, o[3] * inv)};
  u32x2 b = {cvtpk(o[4] * inv, o[5] * inv), cvtpk(o[6] * inv, o[7] * inv)};
  *(u32x2*)(op) = a;
  *(u32x2*)(op + 8) = b;
}

extern "C" void kernel_launch(void* const* d_in, const int* in_sizes, int n_in,
                              void* d_out, int out_size, void* d_ws, size_t ws_size,
                              hipStream_t stream) {
  const u16* x    = (const u16*)d_in[0];
  const u16* wqkv = (const u16*)d_in[1];
  const u16* wo   = (const u16*)d_in[2];
  const u16* w1   = (const u16*)d_in[4];
  const u16* b1   = (const u16*)d_in[5];
  const u16* w2   = (const u16*)d_in[6];
  const u16* b2   = (const u16*)d_in[7];
  u16* out = (u16*)d_out;
  char* ws = (char*)d_ws;
  const size_t T = (size_t)S_ * D_ * 2;
  u16* Qb = (u16*)(ws + 0 * T);
  u16* Kb = (u16*)(ws + 1 * T);
  u16* Vb = (u16*)(ws + 2 * T);
  u16* Ob = (u16*)(ws + 3 * T);
  u16* x1 = (u16*)(ws + 4 * T);
  u16* x2 = (u16*)(ws + 5 * T);
  u16* x3 = (u16*)(ws + 6 * T);
  u16* hb = (u16*)(ws + 7 * T);

  constexpr int LDS_QKV = 96 * 1024 + 96 * 4;
  constexpr int LDS_WO  = 32 * 1024 + 32 * 4;
  constexpr int LDS_F1  = 128 * 1024 + 128 * 4;
  hipFuncSetAttribute((const void*)gemm512_kernel<96, 0>, hipFuncAttributeMaxDynamicSharedMemorySize, LDS_QKV);
  hipFuncSetAttribute((const void*)gemm512_kernel<32, 1>, hipFuncAttributeMaxDynamicSharedMemorySize, LDS_WO);
  hipFuncSetAttribute((const void*)gemm512_kernel<128, 2>, hipFuncAttributeMaxDynamicSharedMemorySize, LDS_F1);
  hipFuncSetAttribute((const void*)ffn2_kernel, hipFuncAttributeMaxDynamicSharedMemorySize, 65536);
  hipFuncSetAttribute((const void*)attn_kernel, hipFuncAttributeMaxDynamicSharedMemorySize, ATT_LDS);

  const u16* xin = x;
  for (int layer = 0; layer < 2; ++layer) {
    u16* xa = layer == 0 ? x1 : x3;
    u16* xo = layer == 0 ? x2 : out;
    gemm512_kernel<96, 0><<<256, 256, LDS_QKV, stream>>>(xin, wqkv, nullptr, Qb, Kb, Vb);
    attn_kernel<<<256, 512, ATT_LDS, stream>>>(Qb, Kb, Vb, Ob);
    gemm512_kernel<32, 1><<<256, 256, LDS_WO, stream>>>(Ob, wo, xin, xa, nullptr, nullptr);
    gemm512_kernel<128, 2><<<224, 256, LDS_F1, stream>>>(xa, w1, b1, hb, nullptr, nullptr);
    ffn2_kernel<<<256, 256, 65536, stream>>>(hb, w2, b2, xa, xo);
    xin = xo;
  }
}
```

```cpp
#include <hip/hip_runtime.h>
#include <hip/hip_bf16.h>
#include <stdint.h>

using bf16x8 = __attribute__((ext_vector_type(8))) short;
using s16x4  = __attribute__((ext_vector_type(4))) short;
using f32x4  = __attribute__((ext_vector_type(4))) float;
using f32x16 = __attribute__((ext_vector_type(16))) float;
using u32x4  = __attribute__((ext_vector_type(4))) unsigned;
using u32x2  = __attribute__((ext_vector_type(2))) unsigned;
typedef unsigned short u16;
typedef short v4i16_t __attribute__((ext_vector_type(4)));
#define LDSP __attribute__((address_space(3)))
#define DI __device__ __forceinline__

constexpr int S_ = 2048, D_ = 512, H_ = 32, F_ = 1792;
constexpr float LN_EPS = 1e-5f;
constexpr float QSCALE = 0.25f * 1.4426950408889634f;

#define MFMA16(a, b, c) __builtin_amdgcn_mfma_f32_16x16x32_bf16((a), (b), (c), 0, 0, 0)
#define MFMA32(a, b, c) __builtin_amdgcn_mfma_f32_32x32x16_bf16((a), (b), (c), 0, 0, 0)

typedef __bf16 bf16x2_t __attribute__((ext_vector_type(2)));
typedef float f32x2_t __attribute__((ext_vector_type(2)));
DI unsigned cvtpk(float lo, float hi) {
  f32x2_t v = {lo, hi};
  return __builtin_bit_cast(unsigned, __builtin_convertvector(v, bf16x2_t));
}
#define PROG(t) do { } while (0)
#define STAMP(kid, i) do { } while (0)
DI float bf_lo(unsigned u) { return __uint_as_float(u << 16); }
DI float bf_hi(unsigned u) { return __uint_as_float(u & 0xffff0000u); }
DI float rbf(float x) { return bf_lo(cvtpk(x, x)); }
DI s16x4 trread(unsigned addr) {
  return __builtin_bit_cast(s16x4, __builtin_amdgcn_ds_read_tr16_b64_v4i16((LDSP v4i16_t*)(uintptr_t)addr));
}
DI bf16x8 cat8(s16x4 lo, s16x4 hi) { return __builtin_shufflevector(lo, hi, 0, 1, 2, 3, 4, 5, 6, 7); }
DI float sum_x16(float x) { auto r = __builtin_amdgcn_permlane16_swap(__float_as_uint(x), __float_as_uint(x), false, false); return __uint_as_float(r[0]) + __uint_as_float(r[1]); }
DI float sum_x32(float x) { auto r = __builtin_amdgcn_permlane32_swap(__float_as_uint(x), __float_as_uint(x), false, false); return __uint_as_float(r[0]) + __uint_as_float(r[1]); }
DI float max_x16(float x) { auto r = __builtin_amdgcn_permlane16_swap(__float_as_uint(x), __float_as_uint(x), false, false); return fmaxf(__uint_as_float(r[0]), __uint_as_float(r[1])); }
DI float max_x32(float x) { auto r = __builtin_amdgcn_permlane32_swap(__float_as_uint(x), __float_as_uint(x), false, false); return fmaxf(__uint_as_float(r[0]), __uint_as_float(r[1])); }
template <int CTRL> DI float dppf(float x) { return __int_as_float(__builtin_amdgcn_update_dpp(0, __float_as_int(x), CTRL, 0xF, 0xF, true)); }
DI float max_row16(float x) {
  x = fmaxf(x, dppf<0xB1>(x)); x = fmaxf(x, dppf<0x4E>(x)); x = fmaxf(x, dppf<0x124>(x)); x = fmaxf(x, dppf<0x128>(x)); return x;
}
DI size_t tiled_off(int row, int k, int KS) {
  return ((size_t)(row >> 4) * KS + (k >> 5)) * 512 + (((k >> 3) & 3) * 16 + (row & 15)) * 8 + (k & 7);
}
DI float gelu_tanh(float x) {
  const float c1 = -2.0f * 0.7978845608028654f * 1.4426950408889634f;
  const float c2 = c1 * 0.044715f;
  float t = x * x;
  float a = x * __builtin_fmaf(t, c2, c1);
  float e = __builtin_amdgcn_exp2f(a);
  return x * __builtin_amdgcn_rcpf(1.0f + e);
}

constexpr int PREP_WU = 384 + 128 + 448 + 448;
constexpr int PREP_WB = PREP_WU / 4;
constexpr int PREP_XB = 32;
constexpr int PREP_CB = 24 + 28;
__global__ __launch_bounds__(256)
void prep_kernel(const u16* __restrict__ x, const u16* __restrict__ wqkv, const u16* __restrict__ wo,
                 const u16* __restrict__ w1, const u16* __restrict__ w2,
                 u16* __restrict__ xt, u16* __restrict__ wqkv_t, u16* __restrict__ wo_t,
                 u16* __restrict__ w1_t, u16* __restrict__ w2_t,
                 float* __restrict__ cs_qkv, float* __restrict__ cs_w1, float* __restrict__ stats)
{
  __shared__ __attribute__((aligned(16))) char smem[16384];
  const int tid = threadIdx.x, lane = tid & 63;
  const int w = __builtin_amdgcn_readfirstlane(tid >> 6);
  const int fr = lane & 15, g = lane >> 4;
  const int bid = blockIdx.x;
  if (bid < PREP_WB) {
    int u = bid * 4 + w;
    const u16* src; u16* dstp; int N, KS, ncb;
    if (u < 384) { src = wqkv; dstp = wqkv_t; N = 1536; KS = 16; ncb = 24; }
    else if (u < 512) { u -= 384; src = wo; dstp = wo_t; N = 512; KS = 16; ncb = 8; }
    else if (u < 960) { u -= 512; src = w1; dstp = w1_t; N = F_; KS = 16; ncb = 28; }
    else { u -= 960; src = w2; dstp = w2_t; N = 512; KS = 56; ncb = 8; }
    const int ks = u / ncb, cb = u % ncb;
    char* stg = smem + w * 4096;
    u32x4 v[4];
#pragma unroll
    for (int i = 0; i < 4; ++i) v[i] = *(const u32x4*)(src + (size_t)(32 * ks + 8 * i + (lane >> 3)) * N + 64 * cb + 8 * (lane & 7));
#pragma unroll
    for (int i = 0; i < 4; ++i) {
      const int r = 8 * i + (lane >> 3), c8 = lane & 7, row16 = r & 15;
      *(u32x4*)(stg + ((r >> 4) * 2 + (c8 >> 2)) * 1024 + row16 * 64 + (((c8 & 3) ^ ((row16 >> 3) << 1)) << 4)) = v[i];
    }
    const unsigned sbase = (unsigned)(uintptr_t)stg;
    const int q4 = (lane & 15) >> 2, p4 = lane & 3;
    const unsigned tb0 = sbase + (g >> 1) * 2048 + (8 * (g & 1) + q4) * 64 + ((g & 1) << 5) + 8 * p4;
    const unsigned tb1 = sbase + (g >> 1) * 2048 + (8 * (g & 1) + q4) * 64 + (((g & 1) ^ 1) << 5) + 8 * p4;
#pragma unroll
    for (int nf = 0; nf < 4; ++nf) {
      const unsigned a = ((nf & 1) ? tb1 : tb0) + (nf >> 1) * 1024;
      const bf16x8 wf = cat8(trread(a), trread(a + 256));
      *(bf16x8*)(dstp + ((size_t)(4 * cb + nf) * KS + ks) * 512 + lane * 8) = wf;
    }
  } else if (bid < PREP_WB + PREP_XB) {
    const int rb = (bid - PREP_WB) * 4 + w;
    bf16x8 v[16];
#pragma unroll
    for (int ks = 0; ks < 16; ++ks) v[ks] = *(const bf16x8*)(x + (size_t)(16 * rb + fr) * D_ + 32 * ks + 8 * g);
#pragma unroll
    for (int ks = 0; ks < 16; ++ks) *(bf16x8*)(xt + ((size_t)rb * 16 + ks) * 512 + lane * 8) = v[ks];
    float s0 = 0.f, q0 = 0.f;
#pragma unroll
    for (int ks = 0; ks < 16; ++ks) {
      const u32x4 u = __builtin_bit_cast(u32x4, v[ks]);
#pragma unroll
      for (int j = 0; j < 4; ++j) { const float a = bf_lo(u[j]), b = bf_hi(u[j]); s0 += a + b; q0 = __builtin_fmaf(a, a, __builtin_fmaf(b, b, q0)); }
    }
    s0 = sum_x32(sum_x16(s0)); q0 = sum_x32(sum_x16(q0));
    if (g == 0) *(f32x2_t*)(stats + (size_t)(16 * rb + fr) * 32) = f32x2_t{s0, q0};
  } else {
    int u = bid - PREP_WB - PREP_XB;
    const u16* src; float* dstp; int N;
    if (u < 24) { src = wqkv; dstp = cs_qkv; N = 1536; } else { u -= 24; src = w1; dstp = cs_w1; N = F_; }
    float acc[8];
#pragma unroll
    for (int j = 0; j < 8; ++j) acc[j] = 0.f;
    u32x4 v[16];
#pragma unroll
    for (int i = 0; i < 16; ++i) v[i] = *(const u32x4*)(src + (size_t)(128 * w + 8 * i + (lane >> 3)) * N + 64 * u + 8 * (lane & 7));
#pragma unroll
    for (int i = 0; i < 16; ++i)
#pragma unroll
      for (int j = 0; j < 4; ++j) { acc[2 * j] += bf_lo(v[i][j]); acc[2 * j + 1] += bf_hi(v[i][j]); }
    float* red = (float*)smem;
#pragma unroll
    for (int j = 0; j < 8; ++j) red[(w * 8 + (lane >> 3)) * 64 + 8 * (lane & 7) + j] = acc[j];
    __syncthreads();
    if (tid < 64) {
      float t = 0.f;
#pragma unroll
      for (int r = 0; r < 32; ++r) t += red[r * 64 + tid];
      dstp[64 * u + tid] = t;
    }
  }
}

DI float dot2bf(unsigned a, unsigned b, float c) {
  return __builtin_amdgcn_fdot2_f32_bf16(__builtin_bit_cast(bf16x2_t, a), __builtin_bit_cast(bf16x2_t, b), c, false);
}
#define GPD 2
template <int BN, int EPI, int NPART>
__global__ __launch_bounds__(512, 2)
void gemm512_kernel(const u16* __restrict__ A, const u16* __restrict__ Wt,
                    const u16* __restrict__ aux,
                    const float* __restrict__ csum,
                    float* __restrict__ stats,
                    u16* __restrict__ out0,
                    u16* __restrict__ out1,
                    u16* __restrict__ out2,
                    float* __restrict__ knorm)
{
  constexpr int N = (EPI == 0) ? 1536 : (EPI == 1 ? 512 : F_);
  constexpr int NT = N / BN;
  constexpr int NF = BN / 16;
  constexpr int WPW = 2 * NF;
  constexpr bool LN = (EPI != 1);
  constexpr int IMG = 16 * NF * 1024;
  extern __shared__ __attribute__((aligned(16))) char smem[];
  float* cs_lds = (float*)(smem + IMG);
  float* b_lds = cs_lds + BN;

  const int tid = threadIdx.x, lane = tid & 63;
  const int w = __builtin_amdgcn_readfirstlane(tid >> 6);
  const int wr = w & 3, kh = w >> 2;
  const int fr = lane & 15, g = lane >> 4;
  STAMP(EPI, 0);

  const int bid = blockIdx.x, xcd = bid & 7, idx = bid >> 3;
  constexpr int PN = NT / 2;
  const int mt = (xcd >> 1) * 4 + idx / PN;
  const int nt = (xcd & 1) * PN + idx % PN;
  const int m0 = mt * 128 + wr * 32;

  u32x4 wst[WPW];
  bf16x8 af[2][8];
  const u16* Ab = A + ((size_t)(m0 >> 4) * 16 + 8 * kh) * 512 + lane * 8;
#pragma unroll
  for (int j = 0; j < WPW; ++j) {
    const int item = wr + 4 * j, nf = item % NF, ksl = item / NF;
    int f;
    if (EPI == 0) f = (nf < 4) ? (4 * nt + nf) : (64 + 2 * nt + nf - 4);
    else f = nt * NF + nf;
    wst[j] = *(const u32x4*)(Wt + ((size_t)f * 16 + 8 * kh + ksl) * 512 + lane * 8);
  }
  auto issueA = [&](int ks) {
#pragma unroll
    for (int i = 0; i < 2; ++i) af[i][ks] = *(const bf16x8*)(Ab + (size_t)(i * 16 + ks) * 512);
  };
#pragma unroll
  for (int ks = 0; ks < GPD; ++ks) issueA(ks);
  const int row = m0 + 16 * kh + fr;
  f32x4 pst0 = f32x4{0.f, 0.f, 0.f, 0.f}, pst1 = f32x4{0.f, 0.f, 0.f, 0.f};
  float pc = 0.f, pb = 0.f;
  u32x2 paux[EPI == 1 ? NF : 1];
  if (LN) {
    const float* sp = stats + (size_t)row * 32;
    if (NPART == 16) { pst0 = *(const f32x4*)(sp + 8 * g); pst1 = *(const f32x4*)(sp + 8 * g + 4); }
    else if (NPART == 8) { pst0 = *(const f32x4*)(sp + 4 * g); }
    else { const f32x2_t t = *(const f32x2_t*)sp; if (g == 0) { pst0[0] = t[0]; pst0[1] = t[1]; } }
    if (tid < BN) {
      if (EPI == 0) pc = csum[tid < 64 ? 64 * nt + tid : 1024 + 32 * nt + tid - 64];
      else { pc = csum[nt * BN + tid]; pb = bf_lo(aux[nt * BN + tid]); }
    }
  } else {
#pragma unroll
    for (int nf = 0; nf < NF; ++nf) paux[nf] = *(const u32x2*)(aux + tiled_off(row, nt * BN + 16 * nf + 4 * g, 16));
  }
  __builtin_amdgcn_sched_barrier(0);

  char* img = smem + kh * 8 * NF * 1024 + lane * 16;
#pragma unroll
  for (int j = 0; j < WPW; ++j) *(u32x4*)(img + (wr + 4 * j) * 1024) = wst[j];
  if (LN && tid < BN) { cs_lds[tid] = pc; b_lds[tid] = pb; }
  __syncthreads();
  STAMP(EPI, 1);
  f32x4 acc[2][NF];
#pragma unroll
  for (int nf = 0; nf < NF; ++nf) { acc[0][nf] = f32x4{0.f, 0.f, 0.f, 0.f}; acc[1][nf] = f32x4{0.f, 0.f, 0.f, 0.f}; }
#pragma unroll
  for (int ks = 0; ks < 8; ++ks) {
    if (ks + GPD < 8) issueA(ks + GPD);
    bf16x8 wf[NF];
#pragma unroll
    for (int nf = 0; nf < NF; ++nf) wf[nf] = *(const bf16x8*)(img + (ks * NF + nf) * 1024);
    __builtin_amdgcn_sched_barrier(0);
#pragma unroll
    for (int nf = 0; nf < NF; ++nf) {
      if (EPI == 0 && nf >= 4) {
        acc[0][nf] = MFMA16(af[0][ks], wf[nf], acc[0][nf]);
        acc[1][nf] = MFMA16(af[1][ks], wf[nf], acc[1][nf]);
      } else {
        acc[0][nf] = MFMA16(wf[nf], af[0][ks], acc[0][nf]);
        acc[1][nf] = MFMA16(wf[nf], af[1][ks], acc[1][nf]);
      }
    }
    __builtin_amdgcn_sched_barrier(0);
  }
  STAMP(EPI, 2);
  __syncthreads();
  f32x4* xch = (f32x4*)smem;
#pragma unroll
  for (int nf = 0; nf < NF; ++nf) xch[(w * NF + nf) * 64 + lane] = kh ? acc[0][nf] : acc[1][nf];
  __syncthreads();
  f32x4 fin[NF];
#pragma unroll
  for (int nf = 0; nf < NF; ++nf) fin[nf] = (kh ? acc[1][nf] : acc[0][nf]) + xch[((w ^ 4) * NF + nf) * 64 + lane];
  float mean = 0.f, rstd = 1.f;
  if (LN) {
    float s = (pst0[0] + pst0[2]) + (pst1[0] + pst1[2]), q = (pst0[1] + pst0[3]) + (pst1[1] + pst1[3]);
    s = sum_x32(sum_x16(s)); q = sum_x32(sum_x16(q));
    mean = s * (1.0f / D_);
    const float var = fmaxf(q * (1.0f / D_) - mean * mean, 0.f);
    rstd = __builtin_amdgcn_rsqf(var + LN_EPS);
  }

  STAMP(EPI, 3);
  if (EPI == 0) {
#pragma unroll
    for (int nf = 0; nf < 4; ++nf) {
      const f32x4 cs = *(const f32x4*)(cs_lds + 16 * nf + 4 * g);
      float y[4];
#pragma unroll
      for (int r = 0; r < 4; ++r) y[r] = rstd * (fin[nf][r] - mean * cs[r]);
      if (nt < 8) {
        const int head = 4 * nt + nf;
        u32x2 v = {cvtpk(y[0] * QSCALE, y[1] * QSCALE), cvtpk(y[2] * QSCALE, y[3] * QSCALE)};
        *(u32x2*)(out0 + ((size_t)head * S_ + row) * 16 + 4 * g) = v;
      } else {
        const int head = 4 * (nt - 8) + nf;
        u32x2 v = {cvtpk(y[0], y[1]), cvtpk(y[2], y[3])};
        *(u32x2*)(out1 + (((size_t)head * 64 + (row >> 5)) * 2 + (g >> 1)) * 256 + (row & 31) * 8 + 4 * (g & 1)) = v;
        float n2 = __builtin_fmaf(y[0], y[0], __builtin_fmaf(y[1], y[1], __builtin_fmaf(y[2], y[2], y[3] * y[3])));
        n2 = max_row16(sum_x32(sum_x16(n2)));
        if (lane == 0) knorm[head * 128 + (row >> 4)] = n2;
      }
    }
    {
      float mu[4], rs[4];
#pragma unroll
      for (int r = 0; r < 4; ++r) { mu[r] = __shfl(mean, 4 * g + r); rs[r] = __shfl(rstd, 4 * g + r); }
#pragma unroll
      for (int nf = 4; nf < 6; ++nf) {
        const float cs = cs_lds[16 * nf + fr];
        float y[4];
#pragma unroll
        for (int r = 0; r < 4; ++r) y[r] = rs[r] * (fin[nf][r] - mu[r] * cs);
        const int head = 2 * nt + (nf - 4);
        u32x2 v = {cvtpk(y[0], y[1]), cvtpk(y[2], y[3])};
        *(u32x2*)(out2 + ((((size_t)head * 64 + (m0 >> 5)) * 2 + kh) * 2 + (g & 1)) * 128 + fr * 8 + 4 * (g >> 1)) = v;
      }
    }
  } else if (EPI == 1) {
    float s0 = 0.f, q0 = 0.f;
#pragma unroll
    for (int nf = 0; nf < NF; ++nf) {
      const int col = nt * BN + 16 * nf + 4 * g;
      const size_t o = tiled_off(row, col, 16);
      const u32x2 xr = paux[nf];
      const float y0 = bf_lo(xr[0]) + rbf(fin[nf][0]), y1 = bf_hi(xr[0]) + rbf(fin[nf][1]);
      const float y2 = bf_lo(xr[1]) + rbf(fin[nf][2]), y3 = bf_hi(xr[1]) + rbf(fin[nf][3]);
      u32x2 v = {cvtpk(y0, y1), cvtpk(y2, y3)};
      *(u32x2*)(out0 + o) = v;
      const float r0 = bf_lo(v[0]), r1 = bf_hi(v[0]), r2 = bf_lo(v[1]), r3 = bf_hi(v[1]);
      s0 += (r0 + r1) + (r2 + r3);
      q0 = __builtin_fmaf(r0, r0, __builtin_fmaf(r1, r1, __builtin_fmaf(r2, r2, __builtin_fmaf(r3, r3, q0))));
    }
    s0 = sum_x32(sum_x16(s0)); q0 = sum_x32(sum_x16(q0));
    if (g == 0) *(f32x2_t*)(stats + (size_t)row * 32 + 2 * nt) = f32x2_t{s0, q0};
  } else {
#pragma unroll
    for (int nf = 0; nf < NF; ++nf) {
      const int col = nt * BN + 16 * nf + 4 * g;
      const f32x4 cs = *(const f32x4*)(cs_lds + 16 * nf + 4 * g);
      const f32x4 b = *(const f32x4*)(b_lds + 16 * nf + 4 * g);
      float y[4];
#pragma unroll
      for (int r = 0; r < 4; ++r) {
        const float t = rbf(rbf(rstd * (fin[nf][r] - mean * cs[r])) + b[r]);
        y[r] = gelu_tanh(t);
      }
      u32x2 v = {cvtpk(y[0], y[1]), cvtpk(y[2], y[3])};
      *(u32x2*)(out0 + tiled_off(row, col, F_ / 32)) = v;
    }
  }
  STAMP(EPI, 4);
}

template <bool OUT_RM>
__global__ __launch_bounds__(256, 1)
void ffn2_kernel(const u16* __restrict__ Hh, const u16* __restrict__ W2t, const u16* __restrict__ b2,
                 const u16* __restrict__ x1, u16* __restrict__ out, float* __restrict__ stats)
{
  extern __shared__ __attribute__((aligned(16))) char smem[];
  const int tid = threadIdx.x, lane = tid & 63;
  const int w = __builtin_amdgcn_readfirstlane(tid >> 6);
  const int fr = lane & 15, g = lane >> 4;
  const int bid = blockIdx.x, xcd = bid & 7, idx = bid >> 3;
  STAMP(4, 0);
  const int mt = xcd * 4 + (idx >> 3), nt = idx & 7;
  const int m0 = mt * 64, n0 = nt * 64;
  constexpr int NS = 14, KS = F_ / 32;

  const u16* Ap = Hh + ((size_t)(m0 >> 4) * KS + w * NS) * 512 + lane * 8;
  const u16* Wp = W2t + ((size_t)(n0 >> 4) * KS + w * NS) * 512 + lane * 8;

  constexpr int PF = 5;
  bf16x8 af[PF + 1][4], wf[PF + 1][4];
  auto issue = [&](int s, int slot) {
#pragma unroll
    for (int i = 0; i < 4; ++i) {
      wf[slot][i] = *(const bf16x8*)(Wp + ((size_t)i * KS + s) * 512);
      af[slot][i] = *(const bf16x8*)(Ap + ((size_t)i * KS + s) * 512);
    }
  };
  f32x4 acc[4][4];
#pragma unroll
  for (int i = 0; i < 4; ++i)
#pragma unroll
    for (int nf = 0; nf < 4; ++nf) acc[i][nf] = f32x4{0.f, 0.f, 0.f, 0.f};
#pragma unroll
  for (int s = 0; s < PF; ++s) issue(s, s);
  const int row = m0 + 16 * w + fr;
  u32x2 pb[4], px[4];
#pragma unroll
  for (int nf = 0; nf < 4; ++nf) {
    pb[nf] = *(const u32x2*)(b2 + n0 + 16 * nf + 4 * g);
    px[nf] = *(const u32x2*)(x1 + tiled_off(row, n0 + 16 * nf + 4 * g, 16));
  }
  __builtin_amdgcn_sched_barrier(0);
#pragma unroll
  for (int s = 0; s < NS; ++s) {
    const int slot = s % (PF + 1);
    if (s + PF < NS) issue(s + PF, (s + PF) % (PF + 1));
    __builtin_amdgcn_sched_barrier(0);
#pragma unroll
    for (int nf = 0; nf < 4; ++nf)
#pragma unroll
      for (int i = 0; i < 4; ++i) acc[i][nf] = MFMA16(wf[slot][nf], af[slot][i], acc[i][nf]);
    __builtin_amdgcn_sched_barrier(0);
  }
  STAMP(4, 1);
  f32x4* red = (f32x4*)smem;
#pragma unroll
  for (int i = 0; i < 4; ++i)
#pragma unroll
    for (int nf = 0; nf < 4; ++nf) red[((w * 4 + i) * 4 + nf) * 64 + lane] = acc[i][nf];
  __syncthreads();
  STAMP(4, 2);
  float s0 = 0.f, q0 = 0.f;
#pragma unroll
  for (int nf = 0; nf < 4; ++nf) {
    f32x4 sum = red[((0 * 4 + w) * 4 + nf) * 64 + lane];
#pragma unroll
    for (int ww = 1; ww < 4; ++ww) sum += red[((ww * 4 + w) * 4 + nf) * 64 + lane];
    const int col = n0 + 16 * nf + 4 * g;
    const u32x2 br = pb[nf];
    const size_t o = tiled_off(row, col, 16);
    const u32x2 xr = px[nf];
    const float y0 = bf_lo(xr[0]) + rbf(rbf(sum[0]) + bf_lo(br[0]));
    const float y1 = bf_hi(xr[0]) + rbf(rbf(sum[1]) + bf_hi(br[0]));
    const float y2 = bf_lo(xr[1]) + rbf(rbf(sum[2]) + bf_lo(br[1]));
    const float y3 = bf_hi(xr[1]) + rbf(rbf(sum[3]) + bf_hi(br[1]));
    u32x2 v = {cvtpk(y0, y1), cvtpk(y2, y3)};
    *(u32x2*)(out + (OUT_RM ? (size_t)row * D_ + col : o)) = v;
    const float r0 = bf_lo(v[0]), r1 = bf_hi(v[0]), r2 = bf_lo(v[1]), r3 = bf_hi(v[1]);
    s0 += (r0 + r1) + (r2 + r3);
    q0 = __builtin_fmaf(r0, r0, __builtin_fmaf(r1, r1, __builtin_fmaf(r2, r2, __builtin_fmaf(r3, r3, q0))));
  }
  if (!OUT_RM) {
    s0 = sum_x32(sum_x16(s0)); q0 = sum_x32(sum_x16(q0));
    if (g == 0) *(f32x2_t*)(stats + (size_t)row * 32 + 2 * nt) = f32x2_t{s0, q0};
  }
  STAMP(4, 3);
}

constexpr int ATT_UNR = 4;
constexpr int ATT_XCH = 65536 * 2 + ATT_UNR * 1024;
constexpr int ATT_LDS = ATT_XCH + 8 * 64 * 48;
DI void attn_tiles(const char* kp, const char* vp, int vstep, int t0, int t1, const bf16x8 qf, const f32x16& negm, f32x16& o) {
  kp += t0 * 1024; vp += (vstep >> 2) * t0;
  for (int t = t0; t < t1; t += ATT_UNR) {
#pragma unroll
    for (int u = 0; u < ATT_UNR; ++u) {
      const bf16x8 kf = *(const bf16x8*)(kp + u * 1024);
      const bf16x8 v0 = *(const bf16x8*)(vp + u * 1024);
      const bf16x8 v1 = *(const bf16x8*)(vp + u * 1024 + 512);
      f32x16 s = MFMA32(kf, qf, negm);
#pragma unroll
      for (int i = 0; i < 16; ++i) s[i] = __builtin_amdgcn_exp2f(s[i]);
      u32x4 p0 = {cvtpk(s[0], s[1]), cvtpk(s[2], s[3]), cvtpk(s[4], s[5]), cvtpk(s[6], s[7])};
      u32x4 p1 = {cvtpk(s[8], s[9]), cvtpk(s[10], s[11]), cvtpk(s[12], s[13]), cvtpk(s[14], s[15])};
      o = MFMA32(v0, __builtin_bit_cast(bf16x8, p0), o);
      o = MFMA32(v1, __builtin_bit_cast(bf16x8, p1), o);
    }
    kp += ATT_UNR * 1024; vp += vstep;
    PROG(t);
  }
}
__global__ __launch_bounds__(1024, 4)
void attn_kernel(const u16* __restrict__ Q, const u16* __restrict__ Kf, const u16* __restrict__ Vf,
                 const float* __restrict__ knorm, u16* __restrict__ O)
{
  extern __shared__ __attribute__((aligned(16))) char smem[];
  const int tid = threadIdx.x, lane = tid & 63;
  const int w = __builtin_amdgcn_readfirstlane(tid >> 6);
  const int wq = w & 7, half = w >> 3;
  const int r = lane & 31, h = lane >> 5;
  const int bid = blockIdx.x;
  const int head = (bid & 7) * 4 + ((bid >> 3) & 3), qt = bid >> 5;
  const int q0 = qt * 256 + wq * 32;
  STAMP(3, 0);

  const int sidx = (tid >> 9) * 2048 + (tid & 511);
  const u32x4* ks = (const u32x4*)(Kf + (size_t)head * 32768) + sidx;
  const u32x4* vs = (const u32x4*)(Vf + (size_t)head * 32768) + sidx;
  u32x4* kd = (u32x4*)smem + sidx;
  u32x4* vd = (u32x4*)(smem + 65536) + sidx;
  u32x4 kr[4], vr[4];
  kr[0] = ks[0]; vr[0] = vs[0];
  const bf16x8 qf = *(const bf16x8*)(Q + ((size_t)head * S_ + q0 + r) * 16 + 8 * h);
  const float kn0 = knorm[head * 128 + lane], kn1 = knorm[head * 128 + 64 + lane];
  if (tid < ATT_UNR * 64) ((u32x4*)(smem + 131072))[tid] = u32x4{0x3F803F80u, 0x3F803F80u, 0x3F803F80u, 0x3F803F80u};
  __builtin_amdgcn_sched_barrier(0);
  kd[0] = kr[0]; vd[0] = vr[0];
#pragma unroll
  for (int c = 1; c < 4; ++c) { kr[c] = ks[c * 512]; vr[c] = vs[c * 512]; }

  float kn = fmaxf(kn0, kn1);
  kn = max_x32(max_x16(max_row16(kn)));
  float qn = 0.f;
  {
    const u32x4 u = __builtin_bit_cast(u32x4, qf);
#pragma unroll
    for (int j = 0; j < 4; ++j) { const float a = bf_lo(u[j]), b = bf_hi(u[j]); qn = __builtin_fmaf(a, a, __builtin_fmaf(b, b, qn)); }
    qn = sum_x32(qn);
  }
  const bool safe = __all(qn * kn <= 3400.0f);

  const char* kp = smem + half * 32768 + lane * 16;
  const char* vp = (r < 16) ? (smem + 65536 + half * 32768 + h * 256 + r * 16) : (smem + 131072 + (r - 16) * 16);
  const int vstep = (r < 16) ? ATT_UNR * 1024 : 0;
  f32x16 zero16;
#pragma unroll
  for (int i = 0; i < 16; ++i) zero16[i] = 0.f;
  f32x16 o = zero16;
  float m = 0.f;
  __syncthreads();
  STAMP(3, 1);
  if (safe) attn_tiles(kp, vp, vstep, 0, 8, qf, zero16, o);
#pragma unroll
  for (int c = 1; c < 4; ++c) { kd[c * 512] = kr[c]; vd[c * 512] = vr[c]; }
  __syncthreads();
  if (safe) attn_tiles(kp, vp, vstep, 8, 32, qf, zero16, o);
  else {
    float m0 = -INFINITY, m1 = -INFINITY;
    for (int t = 0; t < 32; t += 2) {
#pragma unroll
      for (int u = 0; u < 2; ++u) {
        const bf16x8 kf = *(const bf16x8*)(kp + (t + u) * 1024);
        const f32x16 s = MFMA32(kf, qf, zero16);
#pragma unroll
        for (int i = 0; i < 16; i += 4) {
          m0 = __builtin_fmaxf(__builtin_fmaxf(s[i], s[i + 1]), m0);
          m1 = __builtin_fmaxf(__builtin_fmaxf(s[i + 2], s[i + 3]), m1);
        }
      }
    }
    m = max_x32(__builtin_fmaxf(m0, m1));
    f32x16 negm;
#pragma unroll
    for (int i = 0; i < 16; ++i) negm[i] = -m;
    attn_tiles(kp, vp, vstep, 0, 32, qf, negm, o);
  }
  STAMP(3, 2);
  f32x4* xch = (f32x4*)(smem + ATT_XCH) + wq * 192 + lane;
  if (half) {
    xch[0] = f32x4{o[0], o[1], o[2], o[3]};
    xch[64] = f32x4{o[4], o[5], o[6], o[7]};
    xch[128] = f32x4{o[8], m, 0.f, 0.f};
  }
  __syncthreads();
  if (!half) {
    const f32x4 a0 = xch[0], a1 = xch[64], a2 = xch[128];
    const float mm = fmaxf(m, a2[1]);
    const float fa = __builtin_amdgcn_exp2f(m - mm), fb = __builtin_amdgcn_exp2f(a2[1] - mm);
    const float inv = 1.0f / (o[8] * fa + a2[0] * fb);
    const float ia = fa * inv, ib = fb * inv;
    u16* op = O + tiled_off(q0 + r, head * 16 + 4 * h, 16);
    u32x2 a = {cvtpk(o[0] * ia + a0[0] * ib, o[1] * ia + a0[1] * ib), cvtpk(o[2] * ia + a0[2] * ib, o[3] * ia + a0[3] * ib)};
    u32x2 b = {cvtpk(o[4] * ia + a1[0] * ib, o[5] * ia + a1[1] * ib), cvtpk(o[6] * ia + a1[2] * ib, o[7] * ia + a1[3] * ib)};
    *(u32x2*)(op) = a;
    *(u32x2*)(op + 128) = b;
  }
  STAMP(3, 3);
}

extern "C" void kernel_launch(void* const* d_in, const int* in_sizes, int n_in,
                              void* d_out, int out_size, void* d_ws, size_t ws_size,
                              hipStream_t stream) {
  const u16* x    = (const u16*)d_in[0];
  const u16* wqkv = (const u16*)d_in[1];
  const u16* wo   = (const u16*)d_in[2];
  const u16* w1   = (const u16*)d_in[4];
  const u16* b1   = (const u16*)d_in[5];
  const u16* w2   = (const u16*)d_in[6];
  const u16* b2   = (const u16*)d_in[7];
  u16* out = (u16*)d_out;
  char* ws = (char*)d_ws;
  const size_t T = (size_t)S_ * D_ * 2;
  u16* Qb = (u16*)(ws + 0 * T);
  u16* Kb = (u16*)(ws + 1 * T);
  u16* Vb = (u16*)(ws + 2 * T);
  u16* Ob = (u16*)(ws + 3 * T);
  u16* x1 = (u16*)(ws + 4 * T);
  u16* x2 = (u16*)(ws + 5 * T);
  u16* x3 = (u16*)(ws + 6 * T);
  u16* x0 = (u16*)(ws + 7 * T);
  u16* hb = (u16*)(ws + 8 * T);
  u16* wqkv_t = (u16*)(ws + 12 * T);
  u16* wo_t   = (u16*)(ws + 13 * T);
  u16* w1_t   = (u16*)(ws + 14 * T);
  u16* w2_t   = (u16*)(ws + 15 * T);
  float* cs_qkv = (float*)(ws + 16 * T);
  float* cs_w1  = (float*)(ws + 16 * T + 8192);

  float* knrm = (float*)(ws + 16 * T + 16384);
  float* st0 = (float*)(ws + 17 * T);
  float* st1 = (float*)(ws + 17 * T + 262144);
  float* st2 = (float*)(ws + 17 * T + 2 * 262144);
  float* st3 = (float*)(ws + 17 * T + 3 * 262144);

  constexpr int LDS_QKV = 96 * 1024 + 1024;
  constexpr int LDS_WO  = 32 * 1024 + 1024;
  constexpr int LDS_F1  = 112 * 1024 + 1024;
  (void)hipFuncSetAttribute((const void*)gemm512_kernel<96, 0, 1>, hipFuncAttributeMaxDynamicSharedMemorySize, LDS_QKV);
  (void)hipFuncSetAttribute((const void*)gemm512_kernel<96, 0, 8>, hipFuncAttributeMaxDynamicSharedMemorySize, LDS_QKV);
  (void)hipFuncSetAttribute((const void*)gemm512_kernel<32, 1, 1>, hipFuncAttributeMaxDynamicSharedMemorySize, LDS_WO);
  (void)hipFuncSetAttribute((const void*)gemm512_kernel<112, 2, 16>, hipFuncAttributeMaxDynamicSharedMemorySize, LDS_F1);
  (void)hipFuncSetAttribute((const void*)ffn2_kernel<false>, hipFuncAttributeMaxDynamicSharedMemorySize, 65536);
  (void)hipFuncSetAttribute((const void*)ffn2_kernel<true>, hipFuncAttributeMaxDynamicSharedMemorySize, 65536);
  (void)hipFuncSetAttribute((const void*)attn_kernel, hipFuncAttributeMaxDynamicSharedMemorySize, ATT_LDS);

  prep_kernel<<<PREP_WB + PREP_XB + PREP_CB, 256, 0, stream>>>(x, wqkv, wo, w1, w2, x0, wqkv_t, wo_t, w1_t, w2_t, cs_qkv, cs_w1, st0);
  gemm512_kernel<96, 0, 1><<<256, 512, LDS_QKV, stream>>>(x0, wqkv_t, nullptr, cs_qkv, st0, Qb, Kb, Vb, knrm);
  attn_kernel<<<256, 1024, ATT_LDS, stream>>>(Qb, Kb, Vb, knrm, Ob);
  gemm512_kernel<32, 1, 1><<<256, 512, LDS_WO, stream>>>(Ob, wo_t, x0, nullptr, st1, x1, nullptr, nullptr, nullptr);
  gemm512_kernel<112, 2, 16><<<256, 512, LDS_F1, stream>>>(x1, w1_t, b1, cs_w1, st1, hb, nullptr, nullptr, nullptr);
  ffn2_kernel<false><<<256, 256, 65536, stream>>>(hb, w2_t, b2, x1, x2, st2);
  gemm512_kernel<96, 0, 8><<<256, 512, LDS_QKV, stream>>>(x2, wqkv_t, nullptr, cs_qkv, st2, Qb, Kb, Vb, knrm);
  attn_kernel<<<256, 1024, ATT_LDS, stream>>>(Qb, Kb, Vb, knrm, Ob);
  gemm512_kernel<32, 1, 1><<<256, 512, LDS_WO, stream>>>(Ob, wo_t, x2, nullptr, st3, x3, nullptr, nullptr, nullptr);
  gemm512_kernel<112, 2, 16><<<256, 512, LDS_F1, stream>>>(x3, w1_t, b1, cs_w1, st3, hb, nullptr, nullptr, nullptr);
  ffn2_kernel<true><<<256, 256, 65536, stream>>>(hb, w2_t, b2, x3, out, nullptr);
}
```

```cpp
#include <hip/hip_runtime.h>
#include <hip/hip_bf16.h>
#include <stdint.h>

using bf16x8 = __attribute__((ext_vector_type(8))) short;
using s16x4  = __attribute__((ext_vector_type(4))) short;
using f32x4  = __attribute__((ext_vector_type(4))) float;
using f32x16 = __attribute__((ext_vector_type(16))) float;
using u32x4  = __attribute__((ext_vector_type(4))) unsigned;
using u32x2  = __attribute__((ext_vector_type(2))) unsigned;
typedef unsigned short u16;
typedef short v4i16_t __attribute__((ext_vector_type(4)));
#define LDSP __attribute__((address_space(3)))
#define DI __device__ __forceinline__

constexpr int S_ = 2048, D_ = 512, H_ = 32, F_ = 1792;
constexpr float LN_EPS = 1e-5f;
constexpr float QSCALE = 0.25f * 1.4426950408889634f;

#define MFMA16(a, b, c) __builtin_amdgcn_mfma_f32_16x16x32_bf16((a), (b), (c), 0, 0, 0)
#define MFMA32(a, b, c) __builtin_amdgcn_mfma_f32_32x32x16_bf16((a), (b), (c), 0, 0, 0)

typedef __bf16 bf16x2_t __attribute__((ext_vector_type(2)));
typedef float f32x2_t __attribute__((ext_vector_type(2)));
DI unsigned cvtpk(float lo, float hi) {
  f32x2_t v = {lo, hi};
  return __builtin_bit_cast(unsigned, __builtin_convertvector(v, bf16x2_t));
}
#define STOFF(v) do { } while (0)
#define PROG(t) do { } while (0)
#define STAMP(kid, i) do { } while (0)
DI float bf_lo(unsigned u) { return __uint_as_float(u << 16); }
DI float bf_hi(unsigned u) { return __uint_as_float(u & 0xffff0000u); }
DI float rbf(float x) { return bf_lo(cvtpk(x, x)); }
DI s16x4 trread(unsigned addr) {
  return __builtin_bit_cast(s16x4, __builtin_amdgcn_ds_read_tr16_b64_v4i16((LDSP v4i16_t*)(uintptr_t)addr));
}
DI bf16x8 cat8(s16x4 lo, s16x4 hi) { return __builtin_shufflevector(lo, hi, 0, 1, 2, 3, 4, 5, 6, 7); }
DI void st8(void* p, u32x2 v) {
  __hip_atomic_store((unsigned long long*)p, __builtin_bit_cast(unsigned long long, v), __ATOMIC_RELAXED, __HIP_MEMORY_SCOPE_AGENT);
}
DI void st8f(void* p, float a, float b) { st8(p, u32x2{__float_as_uint(a), __float_as_uint(b)}); }
DI void st16(void* p, u32x4 v) {
  asm volatile("global_store_dwordx4 %0, %1, off sc1\n\ts_nop 1" :: "v"(p), "v"(v) : "memory");
}
DI float sum_x16(float x) { auto r = __builtin_amdgcn_permlane16_swap(__float_as_uint(x), __float_as_uint(x), false, false); return __uint_as_float(r[0]) + __uint_as_float(r[1]); }
DI float sum_x32(float x) { auto r = __builtin_amdgcn_permlane32_swap(__float_as_uint(x), __float_as_uint(x), false, false); return __uint_as_float(r[0]) + __uint_as_float(r[1]); }
DI float max_x16(float x) { auto r = __builtin_amdgcn_permlane16_swap(__float_as_uint(x), __float_as_uint(x), false, false); return fmaxf(__uint_as_float(r[0]), __uint_as_float(r[1])); }
DI float max_x32(float x) { auto r = __builtin_amdgcn_permlane32_swap(__float_as_uint(x), __float_as_uint(x), false, false); return fmaxf(__uint_as_float(r[0]), __uint_as_float(r[1])); }
template <int CTRL> DI float dppf(float x) { return __int_as_float(__builtin_amdgcn_update_dpp(0, __float_as_int(x), CTRL, 0xF, 0xF, true)); }
DI float max_row16(float x) {
  x = fmaxf(x, dppf<0xB1>(x)); x = fmaxf(x, dppf<0x4E>(x)); x = fmaxf(x, dppf<0x124>(x)); x = fmaxf(x, dppf<0x128>(x)); return x;
}
DI size_t tiled_off(int row, int k, int KS) {
  return ((size_t)(row >> 4) * KS + (k >> 5)) * 512 + (((k >> 3) & 3) * 16 + (row & 15)) * 8 + (k & 7);
}
DI float gelu_tanh(float x) {
  const float c1 = -2.0f * 0.7978845608028654f * 1.4426950408889634f;
  const float c2 = c1 * 0.044715f;
  float t = x * x;
  float a = x * __builtin_fmaf(t, c2, c1);
  float e = __builtin_amdgcn_exp2f(a);
  return x * __builtin_amdgcn_rcpf(1.0f + e);
}

DI float dot2bf(unsigned a, unsigned b, float c) {
  return __builtin_amdgcn_fdot2_f32_bf16(__builtin_bit_cast(bf16x2_t, a), __builtin_bit_cast(bf16x2_t, b), c, false);
}
template <int NSUB>
DI void tile_unit(const u16* __restrict__ src, u16* __restrict__ dstp, int N, int KS,
                  int ks0, int cb, char* stg, int lane, float (&cs)[4]) {
  const int g = lane >> 4;
  u32x4 v[4 * NSUB];
#pragma unroll
  for (int i = 0; i < 4 * NSUB; ++i) v[i] = *(const u32x4*)(src + (size_t)(32 * ks0 + 8 * i + (lane >> 3)) * N + 64 * cb + 8 * (lane & 7));
  const unsigned sbase = (unsigned)(uintptr_t)stg;
  const int q4 = (lane & 15) >> 2, p4 = lane & 3;
  const unsigned tb0 = sbase + (g >> 1) * 2048 + (8 * (g & 1) + q4) * 64 + ((g & 1) << 5) + 8 * p4;
  const unsigned tb1 = sbase + (g >> 1) * 2048 + (8 * (g & 1) + q4) * 64 + (((g & 1) ^ 1) << 5) + 8 * p4;
#pragma unroll
  for (int nf = 0; nf < 4; ++nf) cs[nf] = 0.f;
#pragma unroll
  for (int s4 = 0; s4 < NSUB; ++s4) {
#pragma unroll
    for (int i = 0; i < 4; ++i) {
      const int r = 8 * i + (lane >> 3), c8 = lane & 7, row16 = r & 15;
      *(u32x4*)(stg + ((r >> 4) * 2 + (c8 >> 2)) * 1024 + row16 * 64 + (((c8 & 3) ^ ((row16 >> 3) << 1)) << 4)) = v[4 * s4 + i];
    }
#pragma unroll
    for (int nf = 0; nf < 4; ++nf) {
      const unsigned a = ((nf & 1) ? tb1 : tb0) + (nf >> 1) * 1024;
      const u32x4 wf = __builtin_bit_cast(u32x4, cat8(trread(a), trread(a + 256)));
      st16(dstp + ((size_t)(4 * cb + nf) * KS + ks0 + s4) * 512 + lane * 8, wf);
#pragma unroll
      for (int j = 0; j < 4; ++j) cs[nf] = dot2bf(wf[j], 0x3F803F80u, cs[nf]);
    }
  }
#pragma unroll
  for (int nf = 0; nf < 4; ++nf) cs[nf] = sum_x32(sum_x16(cs[nf]));
}
DI void stf(float* p, float v) { __hip_atomic_store(p, v, __ATOMIC_RELAXED, __HIP_MEMORY_SCOPE_AGENT); }

struct SideJob { const u16* wo; const u16* w1; const u16* w2; u16* wo_t; u16* w1_t; u16* w2_t; float* cs_w1; };
DI void side_job(const SideJob& sj, int u, int w, char* smem, int lane) {
  float cs[4];
  char* stg = smem + w * 4096;
  if (u < 32) tile_unit<1>(sj.wo, sj.wo_t, 512, 16, 4 * (u & 3) + w, u >> 2, stg, lane, cs);
  else if (u < 144) { u -= 32; tile_unit<1>(sj.w1, sj.w1_t, F_, 16, 4 * (u & 3) + w, u >> 2, stg, lane, cs); }
  else { u -= 144; tile_unit<1>(sj.w2, sj.w2_t, 512, 56, 4 * (u >> 3) + w, u & 7, stg, lane, cs); }
  if (lane < 16) {
#pragma unroll
    for (int nf = 0; nf < 4; ++nf) ((float*)(smem + 24576))[w * 64 + 16 * nf + lane] = cs[nf];
  }
}
DI void side_finish(const SideJob& sj, int u, const char* smem, int lane) {
  if (u >= 32 && u < 144) {
    u -= 32;
    const float* r = (const float*)(smem + 24576);
    stf(sj.cs_w1 + (u & 3) * F_ + 64 * (u >> 2) + lane, (r[lane] + r[64 + lane]) + (r[128 + lane] + r[192 + lane]));
  }
}

typedef __amdgpu_buffer_rsrc_t rsrc_t;
DI rsrc_t mk_rsrc(const void* p, unsigned bytes) { return __builtin_amdgcn_make_buffer_rsrc((void*)p, 0, bytes, 0x00020000); }
DI u32x4 ld16c(rsrc_t r, unsigned off) { return __builtin_bit_cast(u32x4, __builtin_amdgcn_raw_buffer_load_b128(r, off, 0, 16)); }
DI u32x2 ld8c(rsrc_t r, unsigned off) { return __builtin_bit_cast(u32x2, __builtin_amdgcn_raw_buffer_load_b64(r, off, 0, 16)); }
DI void panel_signal(unsigned* cnt) {
  asm volatile("s_waitcnt vmcnt(0)" ::: "memory");
  __syncthreads();
  if (threadIdx.x == 0) __hip_atomic_fetch_add(cnt, 1u, __ATOMIC_RELAXED, __HIP_MEMORY_SCOPE_AGENT);
}
DI void panel_wait(const unsigned* cnt) {
  if (threadIdx.x == 0) {
    int spins = 0;
    while (__hip_atomic_load(cnt, __ATOMIC_RELAXED, __HIP_MEMORY_SCOPE_AGENT) < 16u && ++spins < (1 << 22)) __builtin_amdgcn_s_sleep(1);
  }
}

#define GPD 2
template <int NF, int EPI>
DI void issue_w(u32x4 (&wst)[2 * NF], const u16* __restrict__ Wt, const int nt, const int j0 = 0, const int j1 = 2 * NF) {
  const int lane = threadIdx.x & 63, w = __builtin_amdgcn_readfirstlane(threadIdx.x >> 6), wr = w & 3, kh = w >> 2;
#pragma unroll
  for (int j = 0; j < 2 * NF; ++j) {
    if (j < j0 || j >= j1) continue;
    const int item = wr + 4 * j, nf = item % NF, ksl = item / NF;
    int f;
    if (EPI == 0) f = (nf < 4) ? (4 * nt + nf) : (64 + 2 * nt + nf - 4);
    else f = nt * NF + nf;
    wst[j] = *(const u32x4*)(Wt + ((size_t)f * 16 + 8 * kh + ksl) * 512 + lane * 8);
  }
}
template <int BN, int EPI, int NPART, bool COH, typename NEXT>
DI void gemm_phase(char* smem, const int mt, const int nt, u32x4 (&wst)[BN / 8], NEXT&& next,
                   const u16* __restrict__ A,
                   const u16* __restrict__ aux,
                   const float* __restrict__ csum,
                   float* __restrict__ stats,
                   u16* __restrict__ out0,
                   u16* __restrict__ out1,
                   u16* __restrict__ out2,
                   float* __restrict__ knorm,
                   const unsigned* cnt,
                   const bool aux_rm = false,
                   const u16* __restrict__ Wraw = nullptr, u16* __restrict__ Wt_out = nullptr, float* __restrict__ cs_out = nullptr)
{
  constexpr bool DIRECT = (NPART == 0);
  constexpr int N = (EPI == 0) ? 1536 : (EPI == 1 ? 512 : F_);
  constexpr int NF = BN / 16;
  constexpr int WPW = 2 * NF;
  constexpr bool LN = (EPI != 1);
  constexpr int IMG = 16 * NF * 1024;
  constexpr int APD = (COH || EPI == 1 || DIRECT) ? 8 : GPD;
  float* cs_lds = (float*)(smem + IMG);
  float* b_lds = cs_lds + BN;

  const int tid = threadIdx.x, lane = tid & 63;
  const int w = __builtin_amdgcn_readfirstlane(tid >> 6);
  const int wr = w & 3, kh = w >> 2;
  const int fr = lane & 15, g = lane >> 4;
  const int m0 = mt * 128 + wr * 32;
  const int row = m0 + 16 * kh + fr;
  STAMP(EPI, 0);

  bf16x8 af[2][8];
  const unsigned aoff = (unsigned)((((m0 >> 4) * 16 + 8 * kh) * 512 + lane * 8) * 2);
  const rsrc_t ra = mk_rsrc(A, S_ * D_ * 2);
  const rsrc_t rs_ = mk_rsrc(stats, S_ * 128);
  auto issueA = [&](int ks) {
#pragma unroll
    for (int i = 0; i < 2; ++i) {
      if (COH) af[i][ks] = __builtin_bit_cast(bf16x8, ld16c(ra, aoff + (i * 16 + ks) * 1024));
      else af[i][ks] = *(const bf16x8*)((const char*)A + aoff + (i * 16 + ks) * 1024);
    }
  };
  f32x4 pst0 = f32x4{0.f, 0.f, 0.f, 0.f}, pst1 = f32x4{0.f, 0.f, 0.f, 0.f};
  auto issueStats = [&]() {
    if (COH) {
      const unsigned so = (unsigned)row * 128 + (NPART == 16 ? 32 * g : 16 * g);
      pst0 = __builtin_bit_cast(f32x4, ld16c(rs_, so));
      if (NPART == 16) pst1 = __builtin_bit_cast(f32x4, ld16c(rs_, so + 16));
    } else {
      const float* sp = stats + (size_t)row * 32;
      if (NPART == 16) { pst0 = *(const f32x4*)(sp + 8 * g); pst1 = *(const f32x4*)(sp + 8 * g + 4); }
      else pst0 = *(const f32x4*)(sp + 4 * g);
    }
  };
  float* csp = (float*)(smem + IMG + 1024);
  float* stp = csp + 8 * 96;
  float own_s = 0.f, own_q = 0.f;
  if (DIRECT) {
    char* stg = smem + IMG + 6144 + w * 6144;
    u32x4 wv[2][6], av[16];
#pragma unroll
    for (int k2 = 0; k2 < 2; ++k2) {
      const u16* wr_ = Wraw + (size_t)(32 * (2 * w + k2)) * N;
#pragma unroll
      for (int i = 0; i < 4; ++i) wv[k2][i] = *(const u32x4*)(wr_ + (size_t)(8 * i + (lane >> 3)) * N + 64 * nt + 8 * (lane & 7));
#pragma unroll
      for (int i = 0; i < 2; ++i) wv[k2][4 + i] = *(const u32x4*)(wr_ + (size_t)(16 * i + (lane >> 2)) * N + 1024 + 32 * nt + 8 * (lane & 3));
    }
#pragma unroll
    for (int c = 0; c < 4; ++c)
#pragma unroll
      for (int i = 0; i < 4; ++i) av[4 * c + i] = *(const u32x4*)(A + (size_t)(m0 + 8 * i + (lane >> 3)) * D_ + 256 * kh + 64 * c + 8 * (lane & 7));
    __builtin_amdgcn_sched_barrier(0);
    const unsigned sb = (unsigned)(uintptr_t)stg;
    const int q4 = (lane & 15) >> 2, p4 = lane & 3;
    const unsigned tl = (8 * (g & 1) + q4) * 64 + 8 * p4, sg0 = (g & 1) << 5, sg1 = ((g & 1) ^ 1) << 5;
    bf16x8 ones;
#pragma unroll
    for (int j = 0; j < 8; ++j) ones[j] = (short)0x3F80;
    f32x4 cs[6];
#pragma unroll
    for (int nf = 0; nf < 6; ++nf) cs[nf] = f32x4{0.f, 0.f, 0.f, 0.f};
#pragma unroll
    for (int k2 = 0; k2 < 2; ++k2) {
      const int ks = 2 * w + k2;
#pragma unroll
      for (int i = 0; i < 4; ++i) {
        const int r = 8 * i + (lane >> 3), c8 = lane & 7, row16 = r & 15;
        *(u32x4*)(stg + ((r >> 4) * 2 + (c8 >> 2)) * 1024 + row16 * 64 + (((c8 & 3) ^ ((row16 >> 3) << 1)) << 4)) = wv[k2][i];
      }
#pragma unroll
      for (int i = 0; i < 2; ++i)
        *(u32x4*)(stg + 4096 + i * 1024 + (lane >> 2) * 64 + (((lane & 3) ^ ((lane >> 5) << 1)) << 4)) = wv[k2][4 + i];
#pragma unroll
      for (int nf = 0; nf < 6; ++nf) {
        const unsigned a = (nf < 4) ? sb + (g >> 1) * 2048 + (nf >> 1) * 1024 + tl + ((nf & 1) ? sg1 : sg0)
                                    : sb + 4096 + (g >> 1) * 1024 + tl + ((nf & 1) ? sg1 : sg0);
        const u32x4 wf = __builtin_bit_cast(u32x4, cat8(trread(a), trread(a + 256)));
        *(u32x4*)(smem + (ks * 6 + nf) * 1024 + lane * 16) = wf;
        if (ks == mt) st16(Wt_out + ((size_t)((nf < 4) ? (4 * nt + nf) : (64 + 2 * nt + nf - 4)) * 16 + ks) * 512 + lane * 8, wf);
        cs[nf] = MFMA16(ones, __builtin_bit_cast(bf16x8, wf), cs[nf]);
      }
    }
#pragma unroll
    for (int nf = 0; nf < 6; ++nf) if (g == 0) csp[w * 96 + 16 * nf + fr] = cs[nf][0];
    f32x4 sa[2], qa[2];
#pragma unroll
    for (int i = 0; i < 2; ++i) { sa[i] = f32x4{0.f, 0.f, 0.f, 0.f}; qa[i] = f32x4{0.f, 0.f, 0.f, 0.f}; }
#pragma unroll
    for (int c = 0; c < 4; ++c) {
#pragma unroll
      for (int i = 0; i < 4; ++i) { const int r = 8 * i + (lane >> 3); *(u32x4*)(stg + r * 128 + (((lane & 7) ^ (r & 7)) << 4)) = av[4 * c + i]; }
#pragma unroll
      for (int i = 0; i < 2; ++i)
#pragma unroll
        for (int k2 = 0; k2 < 2; ++k2) {
          const u32x4 u = *(const u32x4*)(stg + (16 * i + fr) * 128 + (((4 * k2 + g) ^ (fr & 7)) << 4));
          af[i][2 * c + k2] = __builtin_bit_cast(bf16x8, u);
          sa[i] = MFMA16(ones, af[i][2 * c + k2], sa[i]);
          qa[i] = MFMA16(af[i][2 * c + k2], af[i][2 * c + k2], qa[i]);
        }
    }
    float ss[2], qq[2];
#pragma unroll
    for (int i = 0; i < 2; ++i) {
      ss[i] = sa[i][0];
      const float d = (fr & 2) ? ((fr & 1) ? qa[i][3] : qa[i][2]) : ((fr & 1) ? qa[i][1] : qa[i][0]);
      qq[i] = sum_x32(sum_x16((g == (fr >> 2)) ? d : 0.f));
    }
    own_s = kh ? ss[1] : ss[0]; own_q = kh ? qq[1] : qq[0];
    if (g == 0) *(f32x2_t*)(stp + (w * 16 + fr) * 2) = f32x2_t{kh ? ss[0] : ss[1], kh ? qq[0] : qq[1]};
  }
  float pc = 0.f, pb = 0.f;
  u32x2 paux[EPI == 1 ? NF : 1];
  if (!COH && !DIRECT) {
#pragma unroll
    for (int ks = 0; ks < APD; ++ks) issueA(ks);
    if (LN) issueStats();
  }
  if (DIRECT) {
  } else if (LN) {
    if (tid < BN) {
      const int cc = (EPI == 0) ? (tid < 64 ? 64 * nt + tid : 1024 + 32 * nt + tid - 64) : nt * BN + tid;
      pc = (csum[cc] + csum[N + cc]) + (csum[2 * N + cc] + csum[3 * N + cc]);
      if (EPI == 2) pb = bf_lo(aux[cc]);
    }
  } else {
#pragma unroll
    for (int nf = 0; nf < NF; ++nf) {
      const int col = nt * BN + 16 * nf + 4 * g;
      paux[nf] = *(const u32x2*)(aux + (aux_rm ? (size_t)row * D_ + col : tiled_off(row, col, 16)));
    }
  }
  __builtin_amdgcn_sched_barrier(0);

  char* img = smem + kh * 8 * NF * 1024 + lane * 16;
  if (!DIRECT) {
#pragma unroll
    for (int j = 0; j < WPW; ++j) *(u32x4*)(img + (wr + 4 * j) * 1024) = wst[j];
    if (LN && tid < BN) { cs_lds[tid] = pc; b_lds[tid] = pb; }
  }
  if (COH) panel_wait(cnt);
  __syncthreads();
  STAMP(EPI, 1);
  if (DIRECT) {
    if (tid < BN) {
      float t = 0.f;
#pragma unroll
      for (int ww = 0; ww < 8; ++ww) t += csp[ww * 96 + tid];
      cs_lds[tid] = t;
      if (mt == 0) {
        const int cc = tid < 64 ? 64 * nt + tid : 1024 + 32 * nt + tid - 64;
        stf(cs_out + cc, t); stf(cs_out + N + cc, 0.f); stf(cs_out + 2 * N + cc, 0.f); stf(cs_out + 3 * N + cc, 0.f);
      }
    }
    const f32x2_t o = *(const f32x2_t*)(stp + ((w ^ 4) * 16 + fr) * 2);
    if (g == 0) pst0 = f32x4{own_s, own_q, o[0], o[1]};
  }
  if (COH) {
#pragma unroll
    for (int ks = 0; ks < 8; ++ks) issueA(ks);
    if (LN) issueStats();
    __builtin_amdgcn_sched_barrier(0);
  }
  f32x4 acc[2][NF];
#pragma unroll
  for (int nf = 0; nf < NF; ++nf) { acc[0][nf] = f32x4{0.f, 0.f, 0.f, 0.f}; acc[1][nf] = f32x4{0.f, 0.f, 0.f, 0.f}; }
#pragma unroll
  for (int ks = 0; ks < 8; ++ks) {
    if (ks + APD < 8) issueA(ks + APD);
    next(ks);
    bf16x8 wf[NF];
#pragma unroll
    for (int nf = 0; nf < NF; ++nf) wf[nf] = *(const bf16x8*)(img + (ks * NF + nf) * 1024);
    __builtin_amdgcn_sched_barrier(0);
#pragma unroll
    for (int nf = 0; nf < NF; ++nf) {
      if (EPI == 0 && nf >= 4) {
        acc[0][nf] = MFMA16(af[0][ks], wf[nf], acc[0][nf]);
        acc[1][nf] = MFMA16(af[1][ks], wf[nf], acc[1][nf]);
      } else {
        acc[0][nf] = MFMA16(wf[nf], af[0][ks], acc[0][nf]);
        acc[1][nf] = MFMA16(wf[nf], af[1][ks], acc[1][nf]);
      }
    }
    __builtin_amdgcn_sched_barrier(0);
  }
  STAMP(EPI, 2);
  next(8);
  __builtin_amdgcn_sched_barrier(0);
  __syncthreads();
  f32x4* xch = (f32x4*)smem;
#pragma unroll
  for (int nf = 0; nf < NF; ++nf) xch[(w * NF + nf) * 64 + lane] = kh ? acc[0][nf] : acc[1][nf];
  __syncthreads();
  f32x4 fin[NF];
#pragma unroll
  for (int nf = 0; nf < NF; ++nf) fin[nf] = (kh ? acc[1][nf] : acc[0][nf]) + xch[((w ^ 4) * NF + nf) * 64 + lane];
  float mean = 0.f, rstd = 1.f;
  if (LN) {
    float s = (pst0[0] + pst0[2]) + (pst1[0] + pst1[2]), q = (pst0[1] + pst0[3]) + (pst1[1] + pst1[3]);
    s = sum_x32(sum_x16(s)); q = sum_x32(sum_x16(q));
    mean = s * (1.0f / D_);
    const float var = fmaxf(q * (1.0f / D_) - mean * mean, 0.f);
    rstd = __builtin_amdgcn_rsqf(var + LN_EPS);
  }
  const float nmr = -mean * rstd;

  STAMP(EPI, 3);
  if (EPI == 0) {
    const float qsc = (nt < 8) ? QSCALE : 1.0f;
    const float qrs = rstd * qsc, qnm = nmr * qsc;
#pragma unroll
    for (int nf = 0; nf < 4; ++nf) {
      const f32x4 cs = *(const f32x4*)(cs_lds + 16 * nf + 4 * g);
      float y[4];
#pragma unroll
      for (int r = 0; r < 4; ++r) y[r] = __builtin_fmaf(fin[nf][r], qrs, qnm * cs[r]);
      u32x2 v = {cvtpk(y[0], y[1]), cvtpk(y[2], y[3])};
      if (nt < 8) {
        const int head = 4 * nt + nf;
        st8(out0 + ((size_t)head * S_ + row) * 16 + 4 * g, v);
      } else {
        const int head = 4 * (nt - 8) + nf;
        st8(out1 + (((size_t)head * 64 + (row >> 5)) * 2 + (g >> 1)) * 256 + (row & 31) * 8 + 4 * (g & 1), v);
        float n2 = __builtin_fmaf(y[0], y[0], __builtin_fmaf(y[1], y[1], __builtin_fmaf(y[2], y[2], y[3] * y[3])));
        n2 = max_row16(sum_x32(sum_x16(n2)));
        if (lane == 0) stf(knorm + head * 128 + (row >> 4), n2);
      }
    }
    {
      float mu[4], rs[4];
#pragma unroll
      for (int r = 0; r < 4; ++r) { mu[r] = __shfl(nmr, 4 * g + r); rs[r] = __shfl(rstd, 4 * g + r); }
#pragma unroll
      for (int nf = 4; nf < 6; ++nf) {
        const float cs = cs_lds[16 * nf + fr];
        float y[4];
#pragma unroll
        for (int r = 0; r < 4; ++r) y[r] = __builtin_fmaf(fin[nf][r], rs[r], mu[r] * cs);
        const int head = 2 * nt + (nf - 4);
        u32x2 v = {cvtpk(y[0], y[1]), cvtpk(y[2], y[3])};
        st8(out2 + ((((size_t)head * 64 + (m0 >> 5)) * 2 + kh) * 2 + (g & 1)) * 128 + fr * 8 + 4 * (g >> 1), v);
      }
    }
  } else if (EPI == 1) {
    float s0 = 0.f, q0 = 0.f;
#pragma unroll
    for (int nf = 0; nf < NF; ++nf) {
      const int col = nt * BN + 16 * nf + 4 * g;
      const u32x2 xr = paux[nf];
      const float y0 = bf_lo(xr[0]) + rbf(fin[nf][0]), y1 = bf_hi(xr[0]) + rbf(fin[nf][1]);
      const float y2 = bf_lo(xr[1]) + rbf(fin[nf][2]), y3 = bf_hi(xr[1]) + rbf(fin[nf][3]);
      u32x2 v = {cvtpk(y0, y1), cvtpk(y2, y3)};
      st8(out0 + tiled_off(row, col, 16), v);
      const float r0 = bf_lo(v[0]), r1 = bf_hi(v[0]), r2 = bf_lo(v[1]), r3 = bf_hi(v[1]);
      s0 += (r0 + r1) + (r2 + r3);
      q0 = __builtin_fmaf(r0, r0, __builtin_fmaf(r1, r1, __builtin_fmaf(r2, r2, __builtin_fmaf(r3, r3, q0))));
    }
    s0 = sum_x32(sum_x16(s0)); q0 = sum_x32(sum_x16(q0));
    if (g == 0) st8f(stats + (size_t)row * 32 + 2 * nt, s0, q0);
  } else {
#pragma unroll
    for (int nf = 0; nf < NF; ++nf) {
      const int col = nt * BN + 16 * nf + 4 * g;
      const f32x4 cs = *(const f32x4*)(cs_lds + 16 * nf + 4 * g);
      const f32x4 b = *(const f32x4*)(b_lds + 16 * nf + 4 * g);
      float y[4];
#pragma unroll
      for (int r = 0; r < 4; ++r) y[r] = gelu_tanh(__builtin_fmaf(fin[nf][r], rstd, __builtin_fmaf(nmr, cs[r], b[r])));
      u32x2 v = {cvtpk(y[0], y[1]), cvtpk(y[2], y[3])};
      st8(out0 + tiled_off(row, col, F_ / 32), v);
    }
  }
  STAMP(EPI, 4);
}

constexpr int LDS_QKV = 96 * 1024 + 6144 + 8 * 6144;
__global__ __launch_bounds__(512, 2)
void qkv_kernel(const u16* __restrict__ x, const u16* __restrict__ W, u16* __restrict__ Wt, float* __restrict__ csum,
                u16* __restrict__ Q, u16* __restrict__ K, u16* __restrict__ V, float* __restrict__ knorm, unsigned* __restrict__ cnt)
{
  extern __shared__ __attribute__((aligned(16))) char smem[];
  const int bid = blockIdx.x, xcd = bid & 7, idx = bid >> 3;
  STOFF(5);
  if (bid < 16) __hip_atomic_store(cnt + bid * 512 + threadIdx.x, 0u, __ATOMIC_RELAXED, __HIP_MEMORY_SCOPE_AGENT);
  u32x4 wst[12];
  gemm_phase<96, 0, 0, false>(smem, (xcd >> 1) * 4 + (idx >> 3), (xcd & 1) * 8 + (idx & 7), wst, [](int) {}, x, nullptr, nullptr, nullptr,
                              Q, K, V, knorm, nullptr, false, W, Wt, csum);
}

constexpr int F2_PF = 7;
DI void issue_w2(bf16x8 (&wf)[F2_PF][4], const u16* __restrict__ W2t, const int nt, const int j0, const int j1) {
  const int lane = threadIdx.x & 63, w = __builtin_amdgcn_readfirstlane(threadIdx.x >> 6);
  const u16* Wp = W2t + ((size_t)(nt * 4) * (F_ / 32) + w * 7) * 512 + lane * 8;
#pragma unroll
  for (int s = 0; s < F2_PF; ++s)
#pragma unroll
    for (int i = 0; i < 4; ++i)
      if (4 * s + i >= j0 && 4 * s + i < j1) wf[s][i] = *(const bf16x8*)(Wp + ((size_t)i * (F_ / 32) + s) * 512);
}
template <typename NEXT>
DI void ffn2_phase(const bool OUT_RM, char* smem, const int m0, const int nt, bf16x8 (&wf)[F2_PF][4], NEXT&& next,
                   const u16* __restrict__ Hh, const u16* __restrict__ W2t, const u16* __restrict__ b2,
                   const u16* __restrict__ x1, u16* __restrict__ out, float* __restrict__ stats, const unsigned* cnt)
{
  const int tid = threadIdx.x, lane = tid & 63;
  const int w = __builtin_amdgcn_readfirstlane(tid >> 6);
  const int fr = lane & 15, g = lane >> 4;
  const int n0 = nt * 64;
  constexpr int NS = 7, KS = F_ / 32, PF = 3;
  STAMP(4, 0);
  const rsrc_t rh = mk_rsrc(Hh, S_ * F_ * 2);
  const rsrc_t rx = mk_rsrc(x1, S_ * D_ * 2);
  const unsigned aoff = (unsigned)((((m0 >> 4) * KS + w * NS) * 512 + lane * 8) * 2);
  bf16x8 af[PF][4];
  auto issueA = [&](int s) {
#pragma unroll
    for (int i = 0; i < 4; ++i) af[s % PF][i] = __builtin_bit_cast(bf16x8, ld16c(rh, aoff + (i * KS + s) * 1024));
  };
  const int fi = w >> 1, fn = 2 * (w & 1);
  const int row = m0 + 16 * fi + fr;
  u32x2 pb[2], px[2];
#pragma unroll
  for (int j = 0; j < 2; ++j) pb[j] = *(const u32x2*)(b2 + n0 + 16 * (fn + j) + 4 * g);
  panel_wait(cnt);
  __syncthreads();
  STAMP(4, 1);
#pragma unroll
  for (int s = 0; s < PF; ++s) issueA(s);
#pragma unroll
  for (int j = 0; j < 2; ++j) px[j] = ld8c(rx, (unsigned)tiled_off(row, n0 + 16 * (fn + j) + 4 * g, 16) * 2);
  f32x4 acc[4][4];
#pragma unroll
  for (int i = 0; i < 4; ++i)
#pragma unroll
    for (int nf = 0; nf < 4; ++nf) acc[i][nf] = f32x4{0.f, 0.f, 0.f, 0.f};
  __builtin_amdgcn_sched_barrier(0);
#pragma unroll
  for (int s = 0; s < NS; ++s) {
#pragma unroll
    for (int nf = 0; nf < 4; ++nf)
#pragma unroll
      for (int i = 0; i < 4; ++i) acc[i][nf] = MFMA16(wf[s][nf], af[s % PF][i], acc[i][nf]);
    __builtin_amdgcn_sched_barrier(0);
    if (s + PF < NS) issueA(s + PF);
    next(s);
    __builtin_amdgcn_sched_barrier(0);
  }
  STAMP(4, 2);
  f32x4* red = (f32x4*)smem;
#pragma unroll
  for (int i = 0; i < 4; ++i)
#pragma unroll
    for (int nf = 0; nf < 4; ++nf) red[((w * 4 + i) * 4 + nf) * 64 + lane] = acc[i][nf];
  __syncthreads();
  STAMP(4, 3);
  float s0 = 0.f, q0 = 0.f;
#pragma unroll
  for (int j = 0; j < 2; ++j) {
    f32x4 sum = red[((0 * 4 + fi) * 4 + fn + j) * 64 + lane];
#pragma unroll
    for (int ww = 1; ww < 8; ++ww) sum += red[((ww * 4 + fi) * 4 + fn + j) * 64 + lane];
    const int col = n0 + 16 * (fn + j) + 4 * g;
    const u32x2 br = pb[j], xr = px[j];
    const float y0 = bf_lo(xr[0]) + rbf(rbf(sum[0]) + bf_lo(br[0]));
    const float y1 = bf_hi(xr[0]) + rbf(rbf(sum[1]) + bf_hi(br[0]));
    const float y2 = bf_lo(xr[1]) + rbf(rbf(sum[2]) + bf_lo(br[1]));
    const float y3 = bf_hi(xr[1]) + rbf(rbf(sum[3]) + bf_hi(br[1]));
    u32x2 v = {cvtpk(y0, y1), cvtpk(y2, y3)};
    st8(out + (OUT_RM ? (size_t)row * D_ + col : tiled_off(row, col, 16)), v);
    const float r0 = bf_lo(v[0]), r1 = bf_hi(v[0]), r2 = bf_lo(v[1]), r3 = bf_hi(v[1]);
    s0 += (r0 + r1) + (r2 + r3);
    q0 = __builtin_fmaf(r0, r0, __builtin_fmaf(r1, r1, __builtin_fmaf(r2, r2, __builtin_fmaf(r3, r3, q0))));
  }
  if (!OUT_RM) {
    s0 = sum_x32(sum_x16(s0)); q0 = sum_x32(sum_x16(q0));
    if (g == 0) st8f(stats + (size_t)row * 32 + 2 * (2 * nt + (w & 1)), s0, q0);
  }
  STAMP(4, 4);
}

struct MlpParams {
  const u16* Ob; const u16* xin; const u16* wo_t; const u16* w1_t; const u16* w2_t; const u16* wqkv_t;
  const u16* b1; const u16* b2; const float* cs_w1; const float* cs_qkv;
  u16* x1; u16* hb; u16* xout; float* st1; float* st2;
  u16* Q; u16* K; u16* V; float* knorm; unsigned* cnt;
};
constexpr int MLP_LDS = 128 * 1024 + 1024;
__global__ __launch_bounds__(512, 2)
void mlp_kernel(MlpParams p)
{
  const bool LAST = (p.Q == nullptr);
  STOFF(LAST ? 8 : 0);
  extern __shared__ __attribute__((aligned(16))) char smem[];
  const int bid = blockIdx.x, xcd = bid & 7, idx = bid >> 3;
  const int panel = xcd * 2 + (idx >> 4), j = idx & 15;
  unsigned* cnt = p.cnt + panel * 256;
  u32x4 w0[4], w1[14], w3[12];
  bf16x8 w2[F2_PF][4];
  issue_w<2, 1>(w0, p.wo_t, j);
  gemm_phase<32, 1, 8, false>(smem, panel, j, w0, [&](int st) { issue_w<7, 2>(w1, p.w1_t, j, 2 * st, 2 * st + 2); },
                              p.Ob, p.xin, nullptr, p.st1, p.x1, nullptr, nullptr, nullptr, nullptr, !LAST);
  panel_signal(cnt);
  gemm_phase<112, 2, 16, true>(smem, panel, j, w1, [&](int st) { issue_w2(w2, p.w2_t, j & 7, 2 * st, st == 8 ? 28 : 2 * st + 2); },
                               p.x1, p.b1, p.cs_w1, p.st1, p.hb, nullptr, nullptr, nullptr, cnt);
  panel_signal(cnt + 64);
  ffn2_phase(LAST, smem, panel * 128 + (j >> 3) * 64, j & 7, w2, [&](int st) { if (!LAST && st >= 3) issue_w<6, 0>(w3, p.wqkv_t, j, 3 * (st - 3), 3 * (st - 3) + 3); },
                   p.hb, p.w2_t, p.b2, p.x1, p.xout, p.st2, cnt + 64);
  if (!LAST) {
    panel_signal(cnt + 128);
    gemm_phase<96, 0, 16, true>(smem, panel, j, w3, [](int) {}, p.xout, nullptr, p.cs_qkv, p.st2, p.Q, p.K, p.V, p.knorm, cnt + 128);
  }
}

constexpr int ATT_UNR = 4;
#define ATT_T0 4
#define ATT_B1 12
#define ATT_B2 20
#define ATT_B3 28
constexpr int ATT_XCH = 65536 * 2 + ATT_UNR * 1024;
constexpr int ATT_FLAG = ATT_XCH + 8 * 64 * 48 + 1024;
constexpr int ATT_LDS = ATT_FLAG + 16;
DI void softmax_pv(f32x16& s, const bf16x8 v0, const bf16x8 v1, f32x16& o) {
#pragma unroll
  for (int i = 0; i < 16; ++i) s[i] = __builtin_amdgcn_exp2f(s[i]);
  const u32x4 p0 = {cvtpk(s[0], s[1]), cvtpk(s[2], s[3]), cvtpk(s[4], s[5]), cvtpk(s[6], s[7])};
  const u32x4 p1 = {cvtpk(s[8], s[9]), cvtpk(s[10], s[11]), cvtpk(s[12], s[13]), cvtpk(s[14], s[15])};
  o = MFMA32(v0, __builtin_bit_cast(bf16x8, p0), o);
  o = MFMA32(v1, __builtin_bit_cast(bf16x8, p1), o);
}
DI void attn_tiles(const char* kp, const char* vp, int vstep, int t0, int t1, const bf16x8 qf, const f32x16& negm, f32x16& o) {
  kp += t0 * 1024; vp += (vstep >> 2) * t0;
  f32x16 sa = MFMA32(*(const bf16x8*)kp, qf, negm), sb;
  bf16x8 kn = *(const bf16x8*)(kp + 1024);
  for (int t = t0; t < t1; t += ATT_UNR) {
#pragma unroll
    for (int u = 0; u < ATT_UNR; u += 2) {
      const bf16x8 va0 = *(const bf16x8*)(vp + u * 1024), va1 = *(const bf16x8*)(vp + u * 1024 + 512);
      const bf16x8 k2 = *(const bf16x8*)(kp + (u + 2) * 1024);
      sb = MFMA32(kn, qf, negm);
      __builtin_amdgcn_sched_barrier(0);
      softmax_pv(sa, va0, va1, o);
      __builtin_amdgcn_sched_barrier(0);
      const bf16x8 vb0 = *(const bf16x8*)(vp + u * 1024 + 1024), vb1 = *(const bf16x8*)(vp + u * 1024 + 1536);
      kn = *(const bf16x8*)(kp + (u + 3) * 1024);
      sa = MFMA32(k2, qf, negm);
      __builtin_amdgcn_sched_barrier(0);
      softmax_pv(sb, vb0, vb1, o);
      __builtin_amdgcn_sched_barrier(0);
    }
    kp += ATT_UNR * 1024; vp += vstep;
    PROG(t);
  }
}
__global__ __launch_bounds__(1024, 4)
void attn_kernel(const u16* __restrict__ Q, const u16* __restrict__ Kf, const u16* __restrict__ Vf,
                 const float* __restrict__ knorm, u16* __restrict__ O, SideJob sj)
{
  extern __shared__ __attribute__((aligned(16))) char smem[];
  const int tid = threadIdx.x, lane = tid & 63;
  const int w = __builtin_amdgcn_readfirstlane(tid >> 6);
  const int wq = w & 7, half = w >> 3;
  const int r = lane & 31, h = lane >> 5;
  const int bid = blockIdx.x;
  const int head = (bid & 7) * 4 + ((bid >> 3) & 3), qt = bid >> 5;
  const int q0 = qt * 256 + wq * 32;
  STOFF(sj.wo ? 0 : 8);
  STAMP(3, 0);

  const int sidx = (tid >> 9) * 2048 + (tid & 511);
  const u32x4* ks = (const u32x4*)(Kf + (size_t)head * 32768) + sidx;
  const u32x4* vs = (const u32x4*)(Vf + (size_t)head * 32768) + sidx;
  u32x4* kd = (u32x4*)smem + sidx;
  u32x4* vd = (u32x4*)(smem + 65536) + sidx;
  u32x4 kr[4], vr[4];
  kr[0] = ks[0]; vr[0] = vs[0];
  const bf16x8 qf = *(const bf16x8*)(Q + ((size_t)head * S_ + q0 + r) * 16 + 8 * h);
  const float kn0 = knorm[head * 128 + lane], kn1 = knorm[head * 128 + 64 + lane];
  if (tid < ATT_UNR * 64) ((u32x4*)(smem + 131072))[tid] = u32x4{0x3F803F80u, 0x3F803F80u, 0x3F803F80u, 0x3F803F80u};
  if (tid == 0) *(volatile int*)(smem + ATT_FLAG) = 0;
  __builtin_amdgcn_sched_barrier(0);
  kd[0] = kr[0]; vd[0] = vr[0];
#pragma unroll
  for (int c = 1; c < 4; ++c) { kr[c] = ks[c * 512]; vr[c] = vs[c * 512]; }

  float kn = fmaxf(kn0, kn1);
  kn = max_x32(max_x16(max_row16(kn)));
  float qn = 0.f;
  {
    const u32x4 u = __builtin_bit_cast(u32x4, qf);
#pragma unroll
    for (int j = 0; j < 4; ++j) { const float a = bf_lo(u[j]), b = bf_hi(u[j]); qn = __builtin_fmaf(a, a, __builtin_fmaf(b, b, qn)); }
    qn = sum_x32(qn);
  }
  const bool safe = __all(qn * kn <= 3400.0f);

  const char* kp = smem + half * 32768 + lane * 16;
  const char* vp = (r < 16) ? (smem + 65536 + half * 32768 + h * 256 + r * 16) : (smem + 131072 + (r - 16) * 16);
  const int vstep = (r < 16) ? ATT_UNR * 1024 : 0;
  f32x16 zero16;
#pragma unroll
  for (int i = 0; i < 16; ++i) zero16[i] = 0.f;
  f32x16 o = zero16;
  float m = 0.f;
  __syncthreads();
  STAMP(3, 1);
  if (safe) attn_tiles(kp, vp, vstep, 0, ATT_T0, qf, zero16, o);
#pragma unroll
  for (int c = 1; c < 4; ++c) { kd[c * 512] = kr[c]; vd[c * 512] = vr[c]; }
  __syncthreads();
  if (sj.wo && w >= 12) {
    side_job(sj, bid, w - 12, smem + ATT_XCH, lane);
    asm volatile("s_waitcnt lgkmcnt(0)" ::: "memory");
    if (lane == 0) atomicAdd((int*)(smem + ATT_FLAG), 1);
  }
  if (safe) {
    __builtin_amdgcn_s_setprio(3);
    attn_tiles(kp, vp, vstep, ATT_T0, ATT_B1, qf, zero16, o);
    __builtin_amdgcn_s_setprio(2);
    attn_tiles(kp, vp, vstep, ATT_B1, ATT_B2, qf, zero16, o);
    __builtin_amdgcn_s_setprio(1);
    attn_tiles(kp, vp, vstep, ATT_B2, ATT_B3, qf, zero16, o);
    __builtin_amdgcn_s_setprio(0);
    attn_tiles(kp, vp, vstep, ATT_B3, 32, qf, zero16, o);
  }
  else {
    float m0 = -INFINITY, m1 = -INFINITY;
    for (int t = 0; t < 32; t += 2) {
#pragma unroll
      for (int u = 0; u < 2; ++u) {
        const bf16x8 kf = *(const bf16x8*)(kp + (t + u) * 1024);
        const f32x16 s = MFMA32(kf, qf, zero16);
#pragma unroll
        for (int i = 0; i < 16; i += 4) {
          m0 = __builtin_fmaxf(__builtin_fmaxf(s[i], s[i + 1]), m0);
          m1 = __builtin_fmaxf(__builtin_fmaxf(s[i + 2], s[i + 3]), m1);
        }
      }
    }
    m = max_x32(__builtin_fmaxf(m0, m1));
    f32x16 negm;
#pragma unroll
    for (int i = 0; i < 16; ++i) negm[i] = -m;
    attn_tiles(kp, vp, vstep, 0, 32, qf, negm, o);
  }
  STAMP(3, 2);
  f32x4* xch = (f32x4*)(smem + ATT_XCH) + wq * 192 + lane;
  if (half) {
    if (sj.wo) { while (*(volatile int*)(smem + ATT_FLAG) < 4) __builtin_amdgcn_s_sleep(1); }
    xch[0] = f32x4{o[0], o[1], o[2], o[3]};
    xch[64] = f32x4{o[4], o[5], o[6], o[7]};
    xch[128] = f32x4{o[8], m, 0.f, 0.f};
  }
  __syncthreads();
  if (sj.wo && w == 12) side_finish(sj, bid, smem + ATT_XCH, lane);
  if (!half) {
    const f32x4 a0 = xch[0], a1 = xch[64], a2 = xch[128];
    const float mm = fmaxf(m, a2[1]);
    const float fa = __builtin_amdgcn_exp2f(m - mm), fb = __builtin_amdgcn_exp2f(a2[1] - mm);
    const float inv = 1.0f / (o[8] * fa + a2[0] * fb);
    const float ia = fa * inv, ib = fb * inv;
    u16* op = O + tiled_off(q0 + r, head * 16 + 4 * h, 16);
    u32x2 a = {cvtpk(o[0] * ia + a0[0] * ib, o[1] * ia + a0[1] * ib), cvtpk(o[2] * ia + a0[2] * ib, o[3] * ia + a0[3] * ib)};
    u32x2 b = {cvtpk(o[4] * ia + a1[0] * ib, o[5] * ia + a1[1] * ib), cvtpk(o[6] * ia + a1[2] * ib, o[7] * ia + a1[3] * ib)};
    st8(op, a);
    st8(op + 128, b);
  }
  STAMP(3, 3);
}

extern "C" void kernel_launch(void* const* d_in, const int* in_sizes, int n_in,
                              void* d_out, int out_size, void* d_ws, size_t ws_size,
                              hipStream_t stream) {
  const u16* x    = (const u16*)d_in[0];
  const u16* wqkv = (const u16*)d_in[1];
  const u16* wo   = (const u16*)d_in[2];
  const u16* w1   = (const u16*)d_in[4];
  const u16* b1   = (const u16*)d_in[5];
  const u16* w2   = (const u16*)d_in[6];
  const u16* b2   = (const u16*)d_in[7];
  u16* out = (u16*)d_out;
  char* ws = (char*)d_ws;
  const size_t T = (size_t)S_ * D_ * 2;
  u16* Qb = (u16*)(ws + 0 * T);
  u16* Kb = (u16*)(ws + 1 * T);
  u16* Vb = (u16*)(ws + 2 * T);
  u16* Ob = (u16*)(ws + 3 * T);
  u16* x1 = (u16*)(ws + 4 * T);
  u16* x2 = (u16*)(ws + 5 * T);
  u16* x3 = (u16*)(ws + 6 * T);
  u16* x0 = (u16*)(ws + 7 * T);
  u16* hb = (u16*)(ws + 8 * T);
  u16* wqkv_t = (u16*)(ws + 12 * T);
  u16* wo_t   = (u16*)(ws + 13 * T);
  u16* w1_t   = (u16*)(ws + 14 * T);
  u16* w2_t   = (u16*)(ws + 15 * T);
  float* cs_qkv = (float*)(ws + 16 * T);
  float* cs_w1  = (float*)(ws + 16 * T + 32768);

  float* knrm = (float*)(ws + 16 * T + 65536);
  float* st0 = (float*)(ws + 17 * T);
  float* st1 = (float*)(ws + 17 * T + 262144);
  float* st2 = (float*)(ws + 17 * T + 2 * 262144);
  float* st3 = (float*)(ws + 17 * T + 3 * 262144);

  u16* hb2 = (u16*)(ws + 18 * T);
  unsigned* cnt = (unsigned*)(ws + 22 * T);

  (void)hipFuncSetAttribute((const void*)qkv_kernel, hipFuncAttributeMaxDynamicSharedMemorySize, LDS_QKV);
  (void)hipFuncSetAttribute((const void*)mlp_kernel, hipFuncAttributeMaxDynamicSharedMemorySize, MLP_LDS);
  (void)hipFuncSetAttribute((const void*)attn_kernel, hipFuncAttributeMaxDynamicSharedMemorySize, ATT_LDS);

  const SideJob sj = {wo, w1, w2, wo_t, w1_t, w2_t, cs_w1};
  const SideJob sj0 = {nullptr, nullptr, nullptr, nullptr, nullptr, nullptr, nullptr};
  const MlpParams p1 = {Ob, x, wo_t, w1_t, w2_t, wqkv_t, b1, b2, cs_w1, cs_qkv, x1, hb, x2, st1, st2, Qb, Kb, Vb, knrm, cnt};
  const MlpParams p2 = {Ob, x2, wo_t, w1_t, w2_t, wqkv_t, b1, b2, cs_w1, cs_qkv, x3, hb2, out, st3, st2, nullptr, Kb, Vb, knrm, cnt + 16 * 256};
  qkv_kernel<<<256, 512, LDS_QKV, stream>>>(x, wqkv, wqkv_t, cs_qkv, Qb, Kb, Vb, knrm, cnt);
  attn_kernel<<<256, 1024, ATT_LDS, stream>>>(Qb, Kb, Vb, knrm, Ob, sj);
  mlp_kernel<<<256, 512, MLP_LDS, stream>>>(p1);
  attn_kernel<<<256, 1024, ATT_LDS, stream>>>(Qb, Kb, Vb, knrm, Ob, sj0);
  mlp_kernel<<<256, 512, MLP_LDS, stream>>>(p2);
}
```
